```python
import torch

def _softmax(x, axis=-1):
    return torch.softmax(x, dim=axis)

def _gelu(x):
    return torch.nn.functional.gelu(x, approximate='tanh')

def _silu(x):
    return torch.nn.functional.silu(x)

def _sigmoid(x):
    return torch.sigmoid(x)

def _layernorm(x, axis=-1, eps=1e-05):
    m = x.mean(dim=axis, keepdim=True)
    v = x.var(dim=axis, keepdim=True, unbiased=False)
    return (x - m) / torch.sqrt(v + eps)

def _scatter_add(tgt, idx, src, axis=0):
    return tgt.clone().index_add_(axis, idx, src)

def _segment_sum(src, seg, n):
    out = torch.zeros((n,) + src.shape[1:], dtype=src.dtype)
    return out.index_add_(0, seg, src)

def _pad(x, pw):
    flat = tuple((v for pair in reversed(pw) for v in pair))
    return torch.nn.functional.pad(x, flat)
kernel_name = 'adaLN_cond__gqa_block_cached__geglu_ffn__decode'

def setup_inputs():

    _w = lambda a: (lambda t: t.to(torch.bfloat16) if t.is_floating_point() else t)(torch.as_tensor(a))

    def _A_setup():
        B, Sq, D = (1, 1, 512)
        Hq, Hkv, Sk, Dh = (32, 8, 16384, 128)
        assert Hq % Hkv == 0, (Hq, Hkv)
        return {'x': _w((lambda *s: torch.randn(*s, dtype=torch.bfloat16) * 0.1)(B, Sq, D)), 'k_cache': _w((lambda *s: torch.randn(*s, dtype=torch.bfloat16) * 0.8)(B, Hkv, Sk, Dh)), 'v_cache': _w((lambda *s: torch.randn(*s, dtype=torch.bfloat16) * 0.8)(B, Hkv, Sk, Dh)), 'wq': _w((lambda *s: torch.randn(*s, dtype=torch.bfloat16) * 0.8)(D, Hq * Dh) * D ** (-0.5)), 'wo': _w((lambda *s: torch.randn(*s, dtype=torch.bfloat16) * 0.8)(Hq * Dh, D) * (Hq * Dh) ** (-0.5))}
    _A_in = _A_setup()

    def _B_setup():
        B, S, D, F = (1, 1, 512, 1792)
        return {'x': _w((lambda *s: torch.randn(*s, dtype=torch.bfloat16) * 0.1)(B, S, D)), 'w_gate': _w((lambda *s: torch.randn(*s, dtype=torch.bfloat16) * 0.1)(D, F)), 'w_up': _w((lambda *s: torch.randn(*s, dtype=torch.bfloat16) * 0.1)(D, F)), 'w_down': _w((lambda *s: torch.randn(*s, dtype=torch.bfloat16) * 0.1)(F, D))}
    _B_in = _B_setup()
    return {'A_x': _A_in['x'], 'A_k_cache': _A_in['k_cache'], 'A_v_cache': _A_in['v_cache'], 'A_wq': _A_in['wq'], 'A_wo': _A_in['wo'], 'B_x': _B_in['x'], 'B_w_gate': _B_in['w_gate'], 'B_w_up': _B_in['w_up'], 'B_w_down': _B_in['w_down'], 'c': _w((lambda *s: torch.randn(*s, dtype=torch.bfloat16) * 0.4)(1, 512) * 8.0), 'w_mod': _w((lambda *s: torch.randn(*s, dtype=torch.bfloat16) * 0.4)(512, 6 * 512) * 512 ** (-0.5) * 8.0)}

def reference(A_x, A_k_cache, A_v_cache, A_wq, A_wo, B_x, B_w_gate, B_w_up, B_w_down, c, w_mod):

    def _A_ref(x, k_cache, v_cache, wq, wo):
        B, Sq, D = x.shape
        Hq, Dh = (32, 128)
        Hkv = k_cache.shape[1]
        rep = Hq // Hkv
        q = (lambda x, *d: x.permute(*d))((x @ wq).reshape(B, Sq, Hq, Dh), 0, 2, 1, 3)
        k = (lambda x, n, axis: torch.repeat_interleave(x, n, dim=axis))(k_cache, rep, axis=1)
        v = (lambda x, n, axis: torch.repeat_interleave(x, n, dim=axis))(v_cache, rep, axis=1)
        scores = torch.einsum('bhqd,bhkd->bhqk', q, k) * Dh ** (-0.5)
        p = _softmax(scores, axis=-1)
        o = torch.einsum('bhqk,bhkd->bhqd', p, v)
        return (lambda x, *d: x.permute(*d))(o, 0, 2, 1, 3).reshape(B, Sq, Hq * Dh) @ wo

    def _B_ref(x, w_gate, w_up, w_down):
        return _gelu(x @ w_gate) * (x @ w_up) @ w_down
    x = A_x
    mod = (_silu(c) @ w_mod).reshape(c.shape[0], 6, 1, -1)
    sa, ca, ga, sb, cb, gb = (mod[:, 0].clone(), mod[:, 1].clone(), mod[:, 2].clone(), mod[:, 3].clone(), mod[:, 4].clone(), mod[:, 5].clone())
    h = _layernorm(x, axis=-1) * (1 + ca) + sa
    x = x + ga * _A_ref(h, A_k_cache, A_v_cache, A_wq, A_wo)
    h = _layernorm(x, axis=-1) * (1 + cb) + sb
    x = x + gb * _B_ref(h, B_w_gate, B_w_up, B_w_down)
    return x
```

```cpp
#include <hip/hip_runtime.h>
#include <hip/hip_bf16.h>
typedef unsigned short u16;
typedef unsigned int u32;
typedef u32 u32x4 __attribute__((ext_vector_type(4)));
typedef float f32x4 __attribute__((ext_vector_type(4)));
typedef short bf16x8 __attribute__((ext_vector_type(8)));
typedef __bf16 bf16v8 __attribute__((ext_vector_type(8)));

__device__ __forceinline__ float bf2f(u32 b) { return __uint_as_float(b << 16); }
__device__ __forceinline__ float rbf(float f) { return (float)(__bf16)f; }
__device__ __forceinline__ u32 f2bf(float f) { return (u32)__builtin_bit_cast(u16, (__bf16)f); }

constexpr int D = 512, HQ = 32, HKV = 8, SK = 16384, DH = 128, FF = 1792;

__device__ __forceinline__ float wave_sum(float v) {
  for (int o = 32; o > 0; o >>= 1) v += __shfl_xor(v, o);
  return v;
}
__device__ void ln_mod(const float* xs, const float* sh, const float* sc, float* h, float* red) {
  int t = threadIdx.x, n = blockDim.x;
  float s = 0;
  for (int i = t; i < D; i += n) s += xs[i];
  s = wave_sum(s);
  if ((t & 63) == 0) red[t >> 6] = s;
  __syncthreads();
  float tot = 0; for (int w = 0; w < n / 64; ++w) tot += red[w];
  float mean = tot / D;
  __syncthreads();
  float v = 0;
  for (int i = t; i < D; i += n) { float d = xs[i] - mean; v += d * d; }
  v = wave_sum(v);
  if ((t & 63) == 0) red[t >> 6] = v;
  __syncthreads();
  float vt = 0; for (int w = 0; w < n / 64; ++w) vt += red[w];
  float m = rbf(mean), var = rbf(vt / D);
  float den = rbf(sqrtf(rbf(var + 1e-5f)));
  for (int i = t; i < D; i += n) {
    float ln = rbf(rbf(xs[i] - m) / den);
    h[i] = rbf(rbf(ln * rbf(1.f + sc[i])) + sh[i]);
  }
  __syncthreads();
}

__device__ void gemv64(const float* a  , const u16* W, int ldw, int K, int col0, float* red  , float* out64) {
  int t = threadIdx.x, cg = t & 7, r = t >> 3;
  float acc[8] = {0};
  for (int k = r; k < K; k += 32) {
    u32x4 w = *(const u32x4*)(W + (size_t)k * ldw + col0 + cg * 8);
    float av = a[k];
    for (int j = 0; j < 4; ++j) { acc[2 * j] += av * bf2f(w[j] & 0xffff); acc[2 * j + 1] += av * bf2f(w[j] >> 16); }
  }
  for (int j = 0; j < 8; ++j) red[r * 64 + cg * 8 + j] = acc[j];
  __syncthreads();
  if (t < 64) { float s = 0; for (int i = 0; i < 32; ++i) s += red[i * 64 + t]; out64[t] = s; }
  __syncthreads();
}

__global__ void __launch_bounds__(256) k_mod(const u16* c, const u16* wmod, float* mod) {
  __shared__ float a[512]; __shared__ float red[32 * 64]; __shared__ float o[64];
  for (int i = threadIdx.x; i < D; i += 256) { float x = bf2f(c[i]); a[i] = rbf(x / (1.f + __expf(-x))); }
  __syncthreads();
  gemv64(a, wmod, 6 * D, D, blockIdx.x * 64, red, o);
  if (threadIdx.x < 64) mod[blockIdx.x * 64 + threadIdx.x] = rbf(o[threadIdx.x]);
}
__global__ void __launch_bounds__(256) k_q(const u16* x, const float* mod, const u16* wq, float* q) {
  __shared__ float xs[512], h[512], red[32 * 64], o[64];
  for (int i = threadIdx.x; i < D; i += 256) xs[i] = bf2f(x[i]);
  __syncthreads();
  ln_mod(xs, mod, mod + 512, h, red);
  gemv64(h, wq, HQ * DH, D, blockIdx.x * 64, red, o);
  if (threadIdx.x < 64) q[blockIdx.x * 64 + threadIdx.x] = rbf(o[threadIdx.x]);
}
__global__ void __launch_bounds__(512) k_attn(const float* q, const u16* K, const u16* V, float* po, float* pml) {
  __shared__ float sacc[8][512]; __shared__ float sm[8][4], sl[8][4];
  int b = blockIdx.x, g = b & 7, s = b >> 3;
  int t = threadIdx.x, w = t >> 6, l = t & 63, lg = l >> 4, lc = l & 15;
  const u16* Kb = K + ((size_t)g * SK + s * 512 + w * 64) * DH;
  const u16* Vb = V + ((size_t)g * SK + s * 512 + w * 64) * DH;
  u32x4 kf[4][4], vf[4][4];
  int krow = 4 * (lc & 3) + (lc >> 2);
  for (int T = 0; T < 4; ++T)
    for (int tt = 0; tt < 4; ++tt) kf[T][tt] = *(const u32x4*)(Kb + (T * 16 + krow) * DH + tt * 32 + lg * 8);
  for (int T = 0; T < 4; ++T)
    for (int r = 0; r < 4; ++r) vf[T][r] = *(const u32x4*)(Vb + (T * 16 + 4 * r + lg) * DH + lc * 8);
  u32x4 qf[4];
  const float* qh = q + (g * 4 + (lc >> 2)) * DH;
  for (int tt = 0; tt < 4; ++tt)
    for (int j = 0; j < 4; ++j) qf[tt][j] = f2bf(qh[tt * 32 + lg * 8 + 2 * j]) | (f2bf(qh[tt * 32 + lg * 8 + 2 * j + 1]) << 16);
  f32x4 sc[4];
  const float scale = 0.08838834764831845f;
  float m = -INFINITY;
  for (int T = 0; T < 4; ++T) {
    f32x4 a = {0, 0, 0, 0};
    for (int tt = 0; tt < 4; ++tt)
      a = __builtin_amdgcn_mfma_f32_16x16x32_bf16(__builtin_bit_cast(bf16v8, kf[T][tt]), __builtin_bit_cast(bf16v8, qf[tt]), a, 0, 0, 0);
    for (int r = 0; r < 4; ++r) { a[r] = rbf(rbf(a[r]) * scale); m = fmaxf(m, a[r]); }
    sc[T] = a;
  }
  m = fmaxf(m, __shfl_xor(m, 16)); m = fmaxf(m, __shfl_xor(m, 32));
  float lsum = 0;
  f32x4 o0 = {0, 0, 0, 0}, o1 = {0, 0, 0, 0};
  const float L2E = 1.4426950408889634f;
  u32 m0 = (lc & 2) ? 0u : ((lc & 1) ? 0xffff0000u : 0x0000ffffu);
  u32 m1 = (lc & 2) ? ((lc & 1) ? 0xffff0000u : 0x0000ffffu) : 0u;
  for (int T = 0; T < 4; ++T)
    for (int r = 0; r < 4; ++r) {
      float p = exp2f((sc[T][r] - m) * L2E);
      u32 pb = f2bf(p);
      lsum += bf2f(pb);
      u32 pp = pb | (pb << 16);
      u32x4 a0 = {pp & m0, pp & m1, 0, 0}, a1 = {0, 0, pp & m0, pp & m1};
      o0 = __builtin_amdgcn_mfma_f32_16x16x32_bf16(__builtin_bit_cast(bf16v8, a0), __builtin_bit_cast(bf16v8, vf[T][r]), o0, 0, 0, 0);
      o1 = __builtin_amdgcn_mfma_f32_16x16x32_bf16(__builtin_bit_cast(bf16v8, a1), __builtin_bit_cast(bf16v8, vf[T][r]), o1, 0, 0, 0);
    }
  lsum += __shfl_xor(lsum, 16); lsum += __shfl_xor(lsum, 32);
  *(f32x4*)&sacc[w][lg * 128 + lc * 8] = o0;
  *(f32x4*)&sacc[w][lg * 128 + lc * 8 + 4] = o1;
  if (lg == 0 && (lc & 3) == 0) { sm[w][lc >> 2] = m; sl[w][lc >> 2] = lsum; }
  __syncthreads();
  int h = t >> 7;
  float M = -INFINITY;
  for (int i = 0; i < 8; ++i) M = fmaxf(M, sm[i][h]);
  float o = 0, L = 0;
  for (int i = 0; i < 8; ++i) { float e = exp2f((sm[i][h] - M) * L2E); o += e * sacc[i][t]; L += e * sl[i][h]; }
  po[((size_t)(g * 4 + h) * 32 + s) * 128 + (t & 127)] = o;
  if ((t & 127) == 0) { pml[((g * 4 + h) * 32 + s) * 2] = M; pml[((g * 4 + h) * 32 + s) * 2 + 1] = L; }
}
__global__ void __launch_bounds__(256) k_wo(const float* po, const float* pml, const u16* wo, float* pw) {
  __shared__ float o[128], red[32 * 64], ov[64];
  int b = blockIdx.x, h = b >> 3, cs = b & 7, t = threadIdx.x;
  const float L2E = 1.4426950408889634f;
  if (t < 128) {
    float M = -INFINITY;
    for (int s = 0; s < 32; ++s) M = fmaxf(M, pml[(h * 32 + s) * 2]);
    float L = 0, a = 0;
    for (int s = 0; s < 32; ++s) { float e = exp2f((pml[(h * 32 + s) * 2] - M) * L2E); L += e * pml[(h * 32 + s) * 2 + 1]; a += e * po[((size_t)h * 32 + s) * 128 + t]; }
    o[t] = rbf(a / L);
  }
  __syncthreads();
  gemv64(o, wo + (size_t)h * 128 * D, D, 128, cs * 64, red, ov);
  if (t < 64) pw[h * 512 + cs * 64 + t] = ov[t];
}
__global__ void __launch_bounds__(256) k_ffn(const u16* x, const float* mod, const float* pw, const u16* wg, const u16* wu, const u16* wd, float* x1o, float* pd) {
  __shared__ float xs[512], h[512], red[32 * 64], gv[64], uv[64], act[64];
  int b = blockIdx.x, t = threadIdx.x;
  for (int i = t; i < D; i += 256) {
    float a = 0; for (int p = 0; p < 32; ++p) a += pw[p * 512 + i];
    xs[i] = rbf(bf2f(x[i]) + rbf(mod[1024 + i] * rbf(a)));
    if (b == 0) x1o[i] = xs[i];
  }
  __syncthreads();
  ln_mod(xs, mod + 1536, mod + 2048, h, red);
  gemv64(h, wg, FF, D, b * 64, red, gv);
  gemv64(h, wu, FF, D, b * 64, red, uv);
  if (t < 64) {
    float gg = rbf(gv[t]);
    float in = 0.7978845608028654f * (gg + 0.044715f * gg * gg * gg);
    float ge = rbf(0.5f * gg * (1.f + tanhf(in)));
    act[t] = rbf(ge * rbf(uv[t]));
  }
  __syncthreads();
  for (int n = t; n < D; n += 256) {
    float a = 0;
    for (int j = 0; j < 64; ++j) a += act[j] * bf2f(wd[(size_t)(b * 64 + j) * D + n]);
    pd[b * 512 + n] = a;
  }
}
__global__ void __launch_bounds__(512) k_fin(const float* mod, const float* x1, const float* pd, u16* out) {
  int t = threadIdx.x;
  float a = 0; for (int p = 0; p < 28; ++p) a += pd[p * 512 + t];
  out[t] = (u16)f2bf(x1[t] + rbf(mod[2560 + t] * rbf(a)));
}

extern "C" void kernel_launch(void* const* d_in, const int* in_sizes, int n_in, void* d_out, int out_size, void* d_ws, size_t ws_size, hipStream_t stream) {
  const u16* x = (const u16*)d_in[0]; const u16* K = (const u16*)d_in[1]; const u16* V = (const u16*)d_in[2];
  const u16* wq = (const u16*)d_in[3]; const u16* wo = (const u16*)d_in[4];
  const u16* wg = (const u16*)d_in[6]; const u16* wu = (const u16*)d_in[7]; const u16* wd = (const u16*)d_in[8];
  const u16* c = (const u16*)d_in[9]; const u16* wmod = (const u16*)d_in[10];
  float* ws = (float*)d_ws;
  float* mod = ws; float* q = ws + 4096; float* pml = ws + 8192; float* po = ws + 16384;
  float* pw = ws + 16384 + 131072; float* x1 = pw + 16384; float* pd = x1 + 512;
  k_mod<<<48, 256, 0, stream>>>(c, wmod, mod);
  k_q<<<64, 256, 0, stream>>>(x, mod, wq, q);
  k_attn<<<256, 512, 0, stream>>>(q, K, V, po, pml);
  k_wo<<<256, 256, 0, stream>>>(po, pml, wo, pw);
  k_ffn<<<28, 256, 0, stream>>>(x, mod, pw, wg, wu, wd, x1, pd);
  k_fin<<<1, 512, 0, stream>>>(mod, x1, pd, (u16*)d_out);
}
```

```cpp
#include <hip/hip_runtime.h>
#include <hip/hip_bf16.h>

typedef unsigned short u16;
typedef unsigned int u32;
typedef unsigned long long u64;
typedef u32 u32x2 __attribute__((ext_vector_type(2)));
typedef u32 u32x4 __attribute__((ext_vector_type(4)));
typedef float f32x2 __attribute__((ext_vector_type(2)));
typedef float f32x4 __attribute__((ext_vector_type(4)));
typedef short s16x4 __attribute__((ext_vector_type(4)));
typedef __bf16 bf16x2 __attribute__((ext_vector_type(2)));
typedef __bf16 bf16x8 __attribute__((ext_vector_type(8)));
typedef __amdgpu_buffer_rsrc_t rsrc_t;
#define LAS __attribute__((address_space(3)))

#define STAMP(k) do {} while (0)
#define STAMPW(k, wv) do {} while (0)
#define DBGCNT(k, v) do {} while (0)
#define ABL 0
#define WIN 4
#define QPOS 12
#define WARM_AT 9
#define POLL_GAP 1
#define POLL_SLOW 12
#define POLL_HI 160
#define P1_HI 24
#define AUX_WARM 0
#define QG_DELAY 4

constexpr int D = 512, SK = 16384, DH = 128, FF = 1792;
constexpr u32 SENT = 0xFFFFFFFFu;
constexpr int AUX_NT = 2, AUX_SC1 = 16;
constexpr int SPIN_CAP = 1 << 16;
constexpr int WS_EPOCH = 0;
constexpr int WS_RDY = 1024;
constexpr int WS_MG = 4096;
constexpr int WS_QG = WS_MG + 1536 * 8;
constexpr int WS_X1 = WS_QG + 16384;
constexpr int WS_PW = WS_X1 + 2048;
constexpr int WS_PO = WS_PW + 65536;
constexpr int WS_PD = WS_PO + 1048576;
constexpr int WS_END = WS_PD + 16 * 224 * 128;
constexpr int OOB = 0x40000000;
constexpr int L_K = 0;
constexpr int L_ACC = 131072;
constexpr int L_Q = L_ACC + 16384;
constexpr int L_H = L_Q + 1024;
constexpr int L_M = L_H + 1024;
constexpr int L_L = L_M + 128;
constexpr int L_R1 = L_L + 128;
constexpr int L_XS = L_R1 + 2048;
constexpr int L_ST = L_XS + 2048;
constexpr int L_R2 = L_ST + 128;
constexpr int L_R3 = L_R2 + 256;
constexpr int L_FM = L_R3 + 256;
constexpr int L_FLAG = L_FM + 512;
constexpr int F_CQ = 0, F_QRDY = 4, F_RDY = 8, F_ISS = 12, F_ATT = 16, F_WARM = 20, F_SM = 24;
constexpr int L_STAMP = L_FLAG + 64;
constexpr int L_END = L_STAMP + 256;
constexpr float L2E = 1.4426950408889634f;

__device__ __forceinline__ float bfl(u32 x) { return __uint_as_float(x << 16); }
__device__ __forceinline__ float bfh(u32 x) { return __uint_as_float(x & 0xffff0000u); }
__device__ __forceinline__ u32 pk2(float lo, float hi) { bf16x2 v = {(__bf16)lo, (__bf16)hi}; return __builtin_bit_cast(u32, v); }
__device__ __forceinline__ float rbf(float f) { return (float)(__bf16)f; }
__device__ __forceinline__ float fmx(float a, float b) { return __builtin_elementwise_maximum(a, b); }
template <int C> __device__ __forceinline__ u32 dppu(u32 x) { return (u32)__builtin_amdgcn_mov_dpp((int)x, C, 0xf, 0xf, true); }
template <int C> __device__ __forceinline__ float dpp(float x) { return __uint_as_float(dppu<C>(__float_as_uint(x))); }
constexpr int XOR1 = 0xB1, XOR2 = 0x4E, ROR4 = 0x124, ROR8 = 0x128;
__device__ __forceinline__ float sw16_add(float a, float b) {
  auto r = __builtin_amdgcn_permlane16_swap(__float_as_uint(a), __float_as_uint(b), false, false);
  return __uint_as_float(r[0]) + __uint_as_float(r[1]);
}
__device__ __forceinline__ float sw32_add(float a, float b) {
  auto r = __builtin_amdgcn_permlane32_swap(__float_as_uint(a), __float_as_uint(b), false, false);
  return __uint_as_float(r[0]) + __uint_as_float(r[1]);
}
__device__ __forceinline__ float sw16_max(float a) {
  auto r = __builtin_amdgcn_permlane16_swap(__float_as_uint(a), __float_as_uint(a), false, false);
  return fmx(__uint_as_float(r[0]), __uint_as_float(r[1]));
}
__device__ __forceinline__ float sw32_max(float a) {
  auto r = __builtin_amdgcn_permlane32_swap(__float_as_uint(a), __float_as_uint(a), false, false);
  return fmx(__uint_as_float(r[0]), __uint_as_float(r[1]));
}
__device__ __forceinline__ float wave_sum2(float a, float b) {
  float x = sw32_add(a, b);
  x = sw16_add(x, x);
  x += dpp<XOR1>(x); x += dpp<XOR2>(x); x += dpp<ROR8>(x); x += dpp<ROR4>(x);
  return x;
}
__device__ __forceinline__ rsrc_t mk(const void* p, u32 bytes) { return __builtin_amdgcn_make_buffer_rsrc((void*)p, 0, bytes, 0x00020000); }
#define LD128(r, v, s, a) __builtin_amdgcn_raw_buffer_load_b128(r, v, s, a)
#define LD64(r, v, s, a) __builtin_amdgcn_raw_buffer_load_b64(r, v, s, a)
#define LD32(r, v, s, a) __builtin_amdgcn_raw_buffer_load_b32(r, v, s, a)
#define ST128(d, r, v, s, a) __builtin_amdgcn_raw_buffer_store_b128(d, r, v, s, a)
#define ST64(d, r, v, s, a) __builtin_amdgcn_raw_buffer_store_b64(d, r, v, s, a)
#define ST32(d, r, v, s, a) __builtin_amdgcn_raw_buffer_store_b32(d, r, v, s, a)
__device__ __forceinline__ u32 canon(float f) { return min(__float_as_uint(f), 0xFFFFFFFEu); }
__device__ __forceinline__ u32x4 canon4(f32x4 v) { return u32x4{canon(v[0]), canon(v[1]), canon(v[2]), canon(v[3])}; }
__device__ __forceinline__ u32 max4(u32x4 v) { return max(max(v[0], v[1]), max(v[2], v[3])); }
__device__ __forceinline__ f32x4 asf(u32x4 v) { return __builtin_bit_cast(f32x4, v); }
__device__ __forceinline__ float rcp(float x) { return __builtin_amdgcn_rcpf(x); }
__device__ __forceinline__ float silu(float x) { return x * rcp(1.f + __expf(-x)); }
__device__ __forceinline__ float dot2(u32 a, u32 b, float c) { return __builtin_amdgcn_fdot2_f32_bf16(__builtin_bit_cast(bf16x2, a), __builtin_bit_cast(bf16x2, b), c, false); }
#define MEMF() asm volatile("" ::: "memory")
#define LGKM0() asm volatile("s_waitcnt lgkmcnt(0)" ::: "memory")
#define VM0() asm volatile("s_waitcnt vmcnt(0)" ::: "memory")
#define BAR() do { LGKM0(); __builtin_amdgcn_s_barrier(); MEMF(); } while (0)
#define SB() __builtin_amdgcn_sched_barrier(0)
#define PACE() asm volatile("s_waitcnt vmcnt(%0)" :: "i"(WIN - 1) : "memory")
#define PIN(x) asm volatile("" : "+v"(x))
__device__ __forceinline__ u32 lds_inc(LAS unsigned char* p) { return __hip_atomic_fetch_add((LAS u32*)p, 1u, __ATOMIC_RELAXED, __HIP_MEMORY_SCOPE_WORKGROUP); }
__device__ __forceinline__ void lds_wait_ge(LAS unsigned char* p, u32 n) {
#pragma nounroll
  for (int sp = 0; sp < SPIN_CAP * 16; ++sp) { if (*(volatile LAS u32*)p >= n) break; __builtin_amdgcn_s_sleep(1); }
  MEMF();
}
__device__ __forceinline__ void ln_fin(float c, float S1, float S2, float& m, float& den) {
  const float d = S1 * (1.f / D);
  const float var = rbf(fmaxf(fmaf(-d, d, S2 * (1.f / D)), 0.f));
  m = rbf(c + d); den = rcp(rbf(__builtin_amdgcn_sqrtf(rbf(var + 1e-5f))));
}
__device__ __forceinline__ void gemv32(const u32 (&wp)[32], const u32 (&hp)[4], float& r0, float& r1) {
  float a[8];
#pragma unroll
  for (int j = 0; j < 8; ++j) a[j] = dot2(wp[24 + j], hp[3], dot2(wp[16 + j], hp[2], dot2(wp[8 + j], hp[1], dot2(wp[j], hp[0], 0.f))));
#pragma unroll
  for (int j = 0; j < 4; ++j) a[j] = sw32_add(a[j], a[j + 4]);
#pragma unroll
  for (int j = 0; j < 2; ++j) a[j] = sw16_add(a[j], a[j + 2]);
#pragma unroll
  for (int j = 0; j < 2; ++j) { a[j] += dpp<ROR8>(a[j]); a[j] += dpp<ROR4>(a[j]); }
  r0 = a[0]; r1 = a[1];
}
#define PAIR_LO(hi, lo) __builtin_amdgcn_perm(hi, lo, 0x05040100u)
#define PAIR_HI(hi, lo) __builtin_amdgcn_perm(hi, lo, 0x07060302u)
#define PH_ISSUE(vo_, so_) asm volatile("s_nop 4\n\t" \
               "buffer_load_dwordx4 v[100:103], %[vo], %[rs], %[so] offen sc1\n\t" \
                : "+{v[100:103]}"(P[0]) : [vo] "v"(vo_), [so] "s"(so_), [rs] "s"(rws) : "memory")
#define PH_WAIT(n) asm volatile("s_waitcnt vmcnt(" #n ")" : "+{v[100:103]}"(P[0]) :: "memory")
#define PH_TIE() asm volatile("" : "+{v[100:103]}"(P[0]))
#define PQ_ISSUE(vo_) asm volatile("s_nop 4\n\t" \
               "buffer_load_dwordx4 v[100:103], %[vo], %[rs], 0 offen sc1\n\t" \
               "buffer_load_dwordx4 v[104:107], %[vo], %[rs], 0 offen offset:1024 sc1\n\t" \
                : "+{v[100:103]}"(P[0]), "+{v[104:107]}"(P[1]) : [vo] "v"(vo_), [rs] "s"(rws) : "memory")
#define PQ_WAIT(n) asm volatile("s_waitcnt vmcnt(" #n ")" : "+{v[100:103]}"(P[0]), "+{v[104:107]}"(P[1]) :: "memory")
#define PQ_TIE() asm volatile("" : "+{v[100:103]}"(P[0]), "+{v[104:107]}"(P[1]))
#define PM_ISSUE(vm_) asm volatile("s_nop 4\n\t" \
               "buffer_load_dwordx4 v[116:119], %[vm], %[rs], 0 offen sc1\n\t" \
                : "+{v[116:119]}"(P[4]) : [vm] "v"(vm_), [rs] "s"(rws) : "memory")
#define PM_WAIT(n) asm volatile("s_waitcnt vmcnt(" #n ")" : "+{v[116:119]}"(P[4]) :: "memory")
#define PM_TIE() asm volatile("" : "+{v[116:119]}"(P[4]))
#define P1_ISSUE(vo_) asm volatile("s_nop 4\n\t" \
               "buffer_load_dwordx4 v[100:103], %[vo], %[rs], 0 offen sc1\n\t" \
               "buffer_load_dwordx4 v[104:107], %[vo], %[rs], 0 offen offset:2048 sc1\n\t" \
               "buffer_load_dwordx4 v[108:111], %[vo], %[rs], %[s1] offen sc1\n\t" \
               "buffer_load_dwordx4 v[112:115], %[vo], %[rs], %[s1] offen offset:2048 sc1\n\t" \
                : "+{v[100:103]}"(P[0]), "+{v[104:107]}"(P[1]), "+{v[108:111]}"(P[2]), "+{v[112:115]}"(P[3]) : [vo] "v"(vo_), [s1] "s"(4096), [rs] "s"(rws) : "memory")
#define P1_WAIT(n) asm volatile("s_waitcnt vmcnt(" #n ")" : "+{v[100:103]}"(P[0]), "+{v[104:107]}"(P[1]), "+{v[108:111]}"(P[2]), "+{v[112:115]}"(P[3]) :: "memory")
#define P1_TIE() asm volatile("" : "+{v[100:103]}"(P[0]), "+{v[104:107]}"(P[1]), "+{v[108:111]}"(P[2]), "+{v[112:115]}"(P[3]))
#define PX_ISSUE(vo_) asm volatile("s_nop 4\n\t" \
               "buffer_load_dwordx4 v[100:103], %[vo], %[rs], 0 offen sc1\n\t" \
               "buffer_load_dwordx4 v[104:107], %[vo], %[rs], 0 offen offset:1024 sc1\n\t" \
                : "+{v[100:103]}"(P[0]), "+{v[104:107]}"(P[1]) : [vo] "v"(vo_), [rs] "s"(rws) : "memory")
#define PX_WAIT(n) asm volatile("s_waitcnt vmcnt(" #n ")" : "+{v[100:103]}"(P[0]), "+{v[104:107]}"(P[1]) :: "memory")
#define PX_TIE() asm volatile("" : "+{v[100:103]}"(P[0]), "+{v[104:107]}"(P[1]))
#define P2_SUMS(r0_, vo_) { int cnt_ = SPIN_CAP / 4, np_, tt_; f32x4 t0_, t1_; asm volatile( \
      "s_nop 4\n\t" \
      "4:\n\t" \
      "buffer_load_dwordx4 v[100:103], %[vo], %[rs], 0 offen sc1\n\t" \
      "buffer_load_dwordx4 v[104:107], %[vo], %[rs], 0 offen offset:1024 sc1\n\t" \
      "buffer_load_dwordx4 v[108:111], %[vo], %[rs], 0 offen offset:2048 sc1\n\t" \
      "buffer_load_dwordx4 v[112:115], %[vo], %[rs], 0 offen offset:3072 sc1\n\t" \
      "1:\n\t" \
      "s_waitcnt vmcnt(0)\n\t" \
      "v_pk_add_f32 v[92:93], v[100:101], v[104:105]\n\t" \
      "v_pk_add_f32 v[94:95], v[102:103], v[106:107]\n\t" \
      "v_pk_add_f32 v[84:85], v[108:109], v[112:113]\n\t" \
      "v_pk_add_f32 v[86:87], v[110:111], v[114:115]\n\t" \
      "v_pk_add_f32 v[92:93], v[92:93], v[84:85]\n\t" \
      "v_pk_add_f32 v[94:95], v[94:95], v[86:87]\n\t" \
      "s_nop 0\n\t" \
      "v_pk_add_f32 v[84:85], v[92:93], v[94:95]\n\t" \
      "s_nop 0\n\t" \
      "v_cmp_u_f32 vcc, v84, v85\n\t" \
      "s_cbranch_vccz 2f\n\t" \
      "s_sub_u32 %[cnt], %[cnt], 1\n\t" \
      "s_cbranch_scc1 2f\n\t" \
      "s_mov_b32 %[np], 0\n\t" \
      "v_cmp_eq_u32 vcc, -1, v100\n\t" \
      "s_bcnt1_i32_b64 %[t], vcc\n\t" \
      "s_add_u32 %[np], %[np], %[t]\n\t" \
      "v_cmp_eq_u32 vcc, -1, v104\n\t" \
      "s_bcnt1_i32_b64 %[t], vcc\n\t" \
      "s_add_u32 %[np], %[np], %[t]\n\t" \
      "v_cmp_eq_u32 vcc, -1, v108\n\t" \
      "s_bcnt1_i32_b64 %[t], vcc\n\t" \
      "s_add_u32 %[np], %[np], %[t]\n\t" \
      "v_cmp_eq_u32 vcc, -1, v112\n\t" \
      "s_bcnt1_i32_b64 %[t], vcc\n\t" \
      "s_add_u32 %[np], %[np], %[t]\n\t" \
      "s_cmp_eq_u32 %[np], 0\n\t" \
      "s_cbranch_scc1 4b\n\t" \
      "s_cmp_gt_u32 %[np], %[hi]\n\t" \
      "s_cbranch_scc0 3f\n\t" \
      "s_sleep %[slow]\n\t" \
      "3:\n\t" \
      "s_sleep %[gap]\n\t" \
      "v_cmp_eq_u32 vcc, -1, v100\n\t" \
      "s_mov_b64 exec, vcc\n\t" \
      "s_cbranch_execz 5f\n\t" \
      "buffer_load_dwordx4 v[100:103], %[vo], %[rs], 0 offen sc1\n\t" \
      "5:\n\t" \
      "s_mov_b64 exec, -1\n\t" \
      "v_cmp_eq_u32 vcc, -1, v104\n\t" \
      "s_mov_b64 exec, vcc\n\t" \
      "s_cbranch_execz 5f\n\t" \
      "buffer_load_dwordx4 v[104:107], %[vo], %[rs], 0 offen offset:1024 sc1\n\t" \
      "5:\n\t" \
      "s_mov_b64 exec, -1\n\t" \
      "v_cmp_eq_u32 vcc, -1, v108\n\t" \
      "s_mov_b64 exec, vcc\n\t" \
      "s_cbranch_execz 5f\n\t" \
      "buffer_load_dwordx4 v[108:111], %[vo], %[rs], 0 offen offset:2048 sc1\n\t" \
      "5:\n\t" \
      "s_mov_b64 exec, -1\n\t" \
      "v_cmp_eq_u32 vcc, -1, v112\n\t" \
      "s_mov_b64 exec, vcc\n\t" \
      "s_cbranch_execz 5f\n\t" \
      "buffer_load_dwordx4 v[112:115], %[vo], %[rs], 0 offen offset:3072 sc1\n\t" \
      "5:\n\t" \
      "s_mov_b64 exec, -1\n\t" \
      "s_branch 1b\n\t" \
      "2:\n\t" "s_nop 0" \
      : "+{v[100:103]}"(P[0]), "+{v[104:107]}"(P[1]), "+{v[108:111]}"(P[2]), "+{v[112:115]}"(P[3]), "=&{v[92:95]}"(r0_), "=&{v[84:87]}"(t0_), "=&{v[88:91]}"(t1_), [cnt] "+&s"(cnt_), [np] "=&s"(np_), [t] "=&s"(tt_) \
      : [vo] "v"(vo_), [rs] "s"(rws), [gap] "i"(POLL_GAP), [slow] "i"(POLL_SLOW), [hi] "i"(POLL_HI) : "vcc", "scc", "memory"); DBGCNT(21, SPIN_CAP / 4 - cnt_ + 1); }
#define P3_SUMS(r0_, vo_, v3_) { int cnt_ = SPIN_CAP / 4, np_, tt_; f32x4 t0_, t1_; asm volatile( \
      "s_nop 4\n\t" \
      "4:\n\t" \
      "buffer_load_dwordx4 v[100:103], %[vo], %[rs], 0 offen sc1\n\t" \
      "buffer_load_dwordx4 v[104:107], %[vo], %[rs], %[s1] offen sc1\n\t" \
      "buffer_load_dwordx4 v[108:111], %[vo], %[rs], %[s2] offen sc1\n\t" \
      "buffer_load_dwordx4 v[112:115], %[v3], %[rs], 0 offen sc1\n\t" \
      "1:\n\t" \
      "s_waitcnt vmcnt(0)\n\t" \
      "v_pk_add_f32 v[92:93], v[100:101], v[104:105]\n\t" \
      "v_pk_add_f32 v[94:95], v[102:103], v[106:107]\n\t" \
      "v_pk_add_f32 v[84:85], v[108:109], v[112:113]\n\t" \
      "v_pk_add_f32 v[86:87], v[110:111], v[114:115]\n\t" \
      "v_pk_add_f32 v[92:93], v[92:93], v[84:85]\n\t" \
      "v_pk_add_f32 v[94:95], v[94:95], v[86:87]\n\t" \
      "s_nop 0\n\t" \
      "v_pk_add_f32 v[84:85], v[92:93], v[94:95]\n\t" \
      "s_nop 0\n\t" \
      "v_cmp_u_f32 vcc, v84, v85\n\t" \
      "s_cbranch_vccz 2f\n\t" \
      "s_sub_u32 %[cnt], %[cnt], 1\n\t" \
      "s_cbranch_scc1 2f\n\t" \
      "s_mov_b32 %[np], 0\n\t" \
      "v_cmp_eq_u32 vcc, -1, v100\n\t" \
      "s_bcnt1_i32_b64 %[t], vcc\n\t" \
      "s_add_u32 %[np], %[np], %[t]\n\t" \
      "v_cmp_eq_u32 vcc, -1, v104\n\t" \
      "s_bcnt1_i32_b64 %[t], vcc\n\t" \
      "s_add_u32 %[np], %[np], %[t]\n\t" \
      "v_cmp_eq_u32 vcc, -1, v108\n\t" \
      "s_bcnt1_i32_b64 %[t], vcc\n\t" \
      "s_add_u32 %[np], %[np], %[t]\n\t" \
      "v_cmp_eq_u32 vcc, -1, v112\n\t" \
      "s_bcnt1_i32_b64 %[t], vcc\n\t" \
      "s_add_u32 %[np], %[np], %[t]\n\t" \
      "s_cmp_eq_u32 %[np], 0\n\t" \
      "s_cbranch_scc1 4b\n\t" \
      "s_cmp_gt_u32 %[np], %[hi]\n\t" \
      "s_cbranch_scc0 3f\n\t" \
      "s_sleep %[slow]\n\t" \
      "3:\n\t" \
      "s_sleep %[gap]\n\t" \
      "v_cmp_eq_u32 vcc, -1, v100\n\t" \
      "s_mov_b64 exec, vcc\n\t" \
      "s_cbranch_execz 5f\n\t" \
      "buffer_load_dwordx4 v[100:103], %[vo], %[rs], 0 offen sc1\n\t" \
      "5:\n\t" \
      "s_mov_b64 exec, -1\n\t" \
      "v_cmp_eq_u32 vcc, -1, v104\n\t" \
      "s_mov_b64 exec, vcc\n\t" \
      "s_cbranch_execz 5f\n\t" \
      "buffer_load_dwordx4 v[104:107], %[vo], %[rs], %[s1] offen sc1\n\t" \
      "5:\n\t" \
      "s_mov_b64 exec, -1\n\t" \
      "v_cmp_eq_u32 vcc, -1, v108\n\t" \
      "s_mov_b64 exec, vcc\n\t" \
      "s_cbranch_execz 5f\n\t" \
      "buffer_load_dwordx4 v[108:111], %[vo], %[rs], %[s2] offen sc1\n\t" \
      "5:\n\t" \
      "s_mov_b64 exec, -1\n\t" \
      "v_cmp_eq_u32 vcc, -1, v112\n\t" \
      "s_mov_b64 exec, vcc\n\t" \
      "s_cbranch_execz 5f\n\t" \
      "buffer_load_dwordx4 v[112:115], %[v3], %[rs], 0 offen sc1\n\t" \
      "5:\n\t" \
      "s_mov_b64 exec, -1\n\t" \
      "s_branch 1b\n\t" \
      "2:\n\t" "s_nop 0" \
      : "+{v[100:103]}"(P[0]), "+{v[104:107]}"(P[1]), "+{v[108:111]}"(P[2]), "+{v[112:115]}"(P[3]), "=&{v[92:95]}"(r0_), "=&{v[84:87]}"(t0_), "=&{v[88:91]}"(t1_), [cnt] "+&s"(cnt_), [np] "=&s"(np_), [t] "=&s"(tt_) \
      : [vo] "v"(vo_), [v3] "v"(v3_), [s1] "s"(8192), [s2] "s"(16384), [rs] "s"(rws), [gap] "i"(POLL_GAP), [slow] "i"(POLL_SLOW), [hi] "i"(POLL_HI) : "vcc", "scc", "memory"); DBGCNT(16, SPIN_CAP / 4 - cnt_ + 1); }
#define KEEP() asm volatile("; keep" :: "{v[100:103]}"(P[0]), "{v[104:107]}"(P[1]), "{v[108:111]}"(P[2]), "{v[112:115]}"(P[3]), "{v[116:119]}"(P[4]), "{v[120:123]}"(P[5]))
#define POOL_INIT() asm volatile("; pool" : "={v[100:103]}"(P[0]), "={v[104:107]}"(P[1]), "={v[108:111]}"(P[2]), "={v[112:115]}"(P[3]), "={v[116:119]}"(P[4]), "={v[120:123]}"(P[5]))

#define POLL(NAME, N, PEND, ...)                     \
  NAME##_ISSUE(__VA_ARGS__); NAME##_WAIT(0);                                                      \
  { bool pend_ = PEND;                                                                            \
    if (__any(pend_)) {                                                                           \
      if (pend_) NAME##_ISSUE(__VA_ARGS__);                                                       \
      __builtin_amdgcn_s_sleep(4);                                                                \
      _Pragma("nounroll") for (int sp = 0; sp < SPIN_CAP; ++sp) {                                 \
        if (pend_) NAME##_ISSUE(__VA_ARGS__);                                                     \
        NAME##_WAIT(N); pend_ = PEND;                                                             \
        if (!__any(pend_)) break;                                                                 \
      } }                                                                                         \
    NAME##_TIE(); }
#define POLLB(NAME, HI, PEND, ...)     \
  NAME##_ISSUE(__VA_ARGS__);                                                                      \
  _Pragma("nounroll") for (int sp = 0; sp < SPIN_CAP; ++sp) {                                     \
    NAME##_WAIT(0);                                                                               \
    DBGCNT(20, sp + 1);                                                                           \
    const bool pend_ = PEND;                                                                      \
    const u64 bal_ = __ballot(pend_);                                                             \
    if (bal_ == 0) break;                                                                         \
    if (__popcll(bal_) > HI) __builtin_amdgcn_s_sleep(POLL_SLOW);                                 \
    __builtin_amdgcn_s_sleep(POLL_GAP);                                                           \
    if (pend_) NAME##_ISSUE(__VA_ARGS__);                                                         \
  }

__global__ void __launch_bounds__(768, 3)
fused_block(const u16* __restrict__ gx, const u16* __restrict__ gk, const u16* __restrict__ gv, const u16* __restrict__ gwq,
            const u16* __restrict__ gwo, const u16* __restrict__ gwg, const u16* __restrict__ gwu, const u16* __restrict__ gwd,
            const u16* __restrict__ gc, const u16* __restrict__ gwm, u16* __restrict__ gout, unsigned char* __restrict__ gws) {
  extern __shared__ __attribute__((aligned(16))) unsigned char smem[];
  LAS unsigned char* lds = (LAS unsigned char*)smem;
  const int b = blockIdx.x, g = b & 7, s = b >> 3;
  const int t = threadIdx.x, w = __builtin_amdgcn_readfirstlane(t >> 6), l = t & 63, lg = l >> 4, lc = l & 15;
  const rsrc_t rws = mk(gws, WS_END);
  STAMPW(0, 0);
  asm volatile("" :: "s"(gx), "s"(gk), "s"(gv), "s"(gwq), "s"(gwo), "s"(gwg), "s"(gwu), "s"(gwd), "s"(gc), "s"(gwm), "s"(gout), "s"(gws));
  if (__builtin_expect(w >= 4, 1)) {
    const int a = w - 4;
    const size_t kvo = ((size_t)g * SK + s * 512) * DH;
    const rsrc_t rk = mk(gk + kvo, (ABL & 256) ? 0 : 131072), rv = mk(gv + kvo, (ABL & 256) ? 0 : 131072);
    {
      const int v0 = lg * 256 + ((lc ^ (4 * lg + 0)) * 16), v1 = lg * 256 + ((lc ^ (4 * lg + 1)) * 16);
      const int v2 = lg * 256 + ((lc ^ (4 * lg + 2)) * 16), v3 = lg * 256 + ((lc ^ (4 * lg + 3)) * 16);
#pragma unroll
      for (int T = 0; T < 4; ++T) {
        const int so = a * 16384 + T * 4096;
        if (T == 0)
          asm volatile("s_mov_b32 m0, %5\n\ts_nop 4\n\t"
                       "buffer_load_dwordx4 %0, %4, %5 offen nt lds\n\t"
                       "buffer_load_dwordx4 %1, %4, %5 offen offset:1024 nt lds\n\t"
                       "s_barrier\n\t"
                       "s_waitcnt vmcnt(%6)\n\tbuffer_load_dwordx4 %2, %4, %5 offen offset:2048 nt lds\n\t"
                       "s_waitcnt vmcnt(%6)\n\tbuffer_load_dwordx4 %3, %4, %5 offen offset:3072 nt lds"
                       :: "v"(v0), "v"(v1), "v"(v2), "v"(v3), "s"(rk), "s"(so), "i"(WIN - 1) : "memory");
        else
          asm volatile("s_mov_b32 m0, %5\n\ts_nop 4\n\t"
                       "s_waitcnt vmcnt(%6)\n\tbuffer_load_dwordx4 %0, %4, %5 offen nt lds\n\t"
                       "s_waitcnt vmcnt(%6)\n\tbuffer_load_dwordx4 %1, %4, %5 offen offset:1024 nt lds\n\t"
                       "s_waitcnt vmcnt(%6)\n\tbuffer_load_dwordx4 %2, %4, %5 offen offset:2048 nt lds\n\t"
                       "s_waitcnt vmcnt(%6)\n\tbuffer_load_dwordx4 %3, %4, %5 offen offset:3072 nt lds"
                       :: "v"(v0), "v"(v1), "v"(v2), "v"(v3), "s"(rk), "s"(so), "i"(WIN - 1) : "memory");
      }
    }
    u32x4 vf[16];
#define VLOAD(i) vf[i] = LD128(rv, l * 16 + ((i) & 3) * 1024, a * 16384 + ((i) >> 2) * 4096, AUX_NT)
#pragma unroll
    for (int i = 0; i < QPOS; ++i) { PACE(); VLOAD(i); if (i == WARM_AT && l == 0) lds_inc(lds + L_FLAG + F_WARM); }
    SB();
    const u32 hm = (lc & 1) ? 0xffff0000u : 0x0000ffffu;
    u32 m0 = (lc & 2) ? 0u : hm, m1 = (lc & 2) ? hm : 0u;
    int ka[4], pubo = WS_PO + (((4 * g + (a & 3)) * 8 + (l >> 3)) * 32 + s) * 128 + (l & 7) * 16;
#pragma unroll
    for (int tt = 0; tt < 4; ++tt) { ka[tt] = L_K + a * 16384 + (4 * (lc & 3) + (lc >> 2)) * 256 + (((4 * tt + lg) ^ lc) * 16); PIN(ka[tt]); }
    PIN(m0); PIN(m1); PIN(pubo);
    STAMPW(5, 4);
    lds_wait_ge(lds + L_FLAG + F_QRDY, 1u);
#pragma unroll
    for (int i = QPOS; i < 16; ++i) VLOAD(i);
    SB();
    if (l == 0) lds_inc(lds + L_FLAG + F_ISS);
    u32x4 qf[4];
#pragma unroll
    for (int tt = 0; tt < 4; ++tt) qf[tt] = *(LAS u32x4*)(lds + L_Q + (lc >> 2) * 256 + tt * 64 + lg * 16);
    asm volatile("s_waitcnt vmcnt(16)" ::: "memory");
    STAMPW(6, 4);
    f32x4 sc;
    {
      f32x4 c[4];
#pragma unroll
      for (int T = 0; T < 4; ++T) {
        c[T] = f32x4{0.f, 0.f, 0.f, 0.f};
#pragma unroll
        for (int tt = 0; tt < 4; ++tt)
          c[T] = __builtin_amdgcn_mfma_f32_16x16x32_bf16(__builtin_bit_cast(bf16x8, *(LAS u32x4*)(lds + ka[tt] + T * 4096)), __builtin_bit_cast(bf16x8, qf[tt]), c[T], 0, 0, 0);
      }
      const f32x4 lo = (l & 1) ? c[1] : c[0], hi = (l & 1) ? c[3] : c[2];
      sc = (l & 2) ? hi : lo;
    }
#pragma unroll
    for (int r = 0; r < 4; ++r) sc[r] = rbf(rbf(sc[r]) * 0.08838834764831845f);
    float mw = fmx(fmx(sc[0], sc[1]), fmx(sc[2], sc[3]));
    mw = fmx(mw, dpp<XOR1>(mw)); mw = fmx(mw, dpp<XOR2>(mw)); mw = sw32_max(sw16_max(mw));
    u32 pp[4];
    float lw = 0.f;
#pragma unroll
    for (int r = 0; r < 4; ++r) { const float p = __builtin_amdgcn_exp2f((sc[r] - mw) * L2E); lw += p; pp[r] = pk2(p, p); }
    f32x4 o0 = {0.f, 0.f, 0.f, 0.f}, o1 = {0.f, 0.f, 0.f, 0.f};
#define PV(i, Q) { const u32 x_ = dppu<Q>(pp[(i) & 3]); const u32x2 pa_ = {x_ & m0, x_ & m1};                                                         \
      o0 = __builtin_amdgcn_mfma_f32_16x16x16bf16_1k(__builtin_bit_cast(s16x4, pa_), __builtin_bit_cast(s16x4, u32x2{vf[i][0], vf[i][1]}), o0, 0, 0, 0); \
      o1 = __builtin_amdgcn_mfma_f32_16x16x16bf16_1k(__builtin_bit_cast(s16x4, pa_), __builtin_bit_cast(s16x4, u32x2{vf[i][2], vf[i][3]}), o1, 0, 0, 0); }
    PV(0, 0x00) PV(1, 0x00) PV(2, 0x00) PV(3, 0x00)
    lw += dpp<XOR1>(lw); lw += dpp<XOR2>(lw); lw = sw16_add(lw, lw); lw = sw32_add(lw, lw);
    if (lg == 0 && (lc & 3) == 0) { *(LAS float*)(lds + L_M + (lc >> 2) * 32 + a * 4) = mw; *(LAS float*)(lds + L_L + (lc >> 2) * 32 + a * 4) = lw; }
    LGKM0();
    if (l == 0) lds_inc(lds + L_FLAG + F_SM);
    PV(4, 0x55) PV(5, 0x55) PV(6, 0x55) PV(7, 0x55) PV(8, 0xAA) PV(9, 0xAA) PV(10, 0xAA) PV(11, 0xAA)
    float e[8], M = 0.f, L = 0.f;
    if (a < 4) {
      lds_wait_ge(lds + L_FLAG + F_SM, 8u);
      const f32x4 ma = *(LAS f32x4*)(lds + L_M + a * 32), mb = *(LAS f32x4*)(lds + L_M + a * 32 + 16);
      const f32x4 la = *(LAS f32x4*)(lds + L_L + a * 32), lb = *(LAS f32x4*)(lds + L_L + a * 32 + 16);
      M = fmx(fmx(fmx(ma[0], ma[1]), fmx(ma[2], ma[3])), fmx(fmx(mb[0], mb[1]), fmx(mb[2], mb[3])));
#pragma unroll
      for (int i = 0; i < 8; ++i) {
        e[i] = __builtin_amdgcn_exp2f(((i < 4 ? ma[i & 3] : mb[i & 3]) - M) * L2E);
        L = fmaf(e[i], i < 4 ? la[i & 3] : lb[i & 3], L);
        PIN(e[i]);
      }
      PIN(L);
    }
    SB();
    PV(12, 0xFF) PV(13, 0xFF) PV(14, 0xFF) PV(15, 0xFF)
    __builtin_amdgcn_s_setprio(3);
    *(LAS f32x4*)(lds + L_ACC + a * 2048 + lg * 512 + lc * 32) = o0;
    *(LAS f32x4*)(lds + L_ACC + a * 2048 + lg * 512 + lc * 32 + 16) = o1;
    LGKM0();
    if (l == 0) lds_inc(lds + L_FLAG + F_ATT);
    STAMPW(7, 11); STAMPW(23, 4);
    if (a < 4) {
      const int ao = L_ACC + a * 512 + ((l >> 3) * 4 + (l & 3)) * 16;
      lds_wait_ge(lds + L_FLAG + F_ATT, 8u);
      f32x4 oa = e[0] * *(LAS f32x4*)(lds + ao), ob = e[1] * *(LAS f32x4*)(lds + ao + 2048);
#pragma unroll
      for (int i = 2; i < 8; i += 2) { oa += e[i] * *(LAS f32x4*)(lds + ao + i * 2048); ob += e[i + 1] * *(LAS f32x4*)(lds + ao + (i + 1) * 2048); }
      f32x4 o = oa + ob;
      if (l & 4) o = f32x4{M, L, 0.f, 0.f};
      ST128(canon4(o), rws, pubo, 0, AUX_SC1);
      STAMPW(8, 4);
    }
  } else {
    const rsrc_t rx = mk(gx, 1024);
    const int hh = 4 * g + (s >> 3), cs = s & 7, uj = 28 * g + s, fin = 4 * g + s;
    const bool ffn = s < 28, red = s == 28 || s == 29;
    const int rid = 2 * g + (s & 1);
    u32 tag, arr;
    u32x4 P[6];
    POOL_INIT();
#define TAGS(v) ((v[1] ^ tag) | (v[3] ^ tag))
    {
      const bool qjob = s >= 16, mjob = s >= 4 && s < 16;
      const int job = qjob ? 16 * g + (s - 16) : 12 * g + (s - 4);
      const int pitch = qjob ? 8192 : 6144;
      const rsrc_t rc = mk(gc, 1024), rw = mk(qjob ? gwq : gwm, qjob ? ((ABL & 8) ? 0 : 512 * 8192) : (mjob && !(ABL & 4) ? 512 * 6144 : 0));
      u32x4 wv[8];
#pragma unroll
      for (int i = 0; i < 8; ++i) wv[i] = LD128(rw, (l >> 2) * 2 * pitch + (l & 3) * 16 + job * 64, (128 * w + (i >> 1) * 32 + (i & 1)) * pitch, AUX_NT);
      SB();
      u32 hp[4];
#pragma unroll
      for (int i = 0; i < 4; ++i) hp[i] = LD32(rc, (l >> 2) * 4 + i * 64, w * 256, 0);
      const u32 ep = LD32(rws, WS_EPOCH, 0, AUX_SC1);
      const u32x4 xv = LD128(rx, l * 16, 0, 0);
      const u32x2 xo = LD64(rx, (l & 31) * 8, w * 256, 0);
      SB();
      if (t < 8) *(volatile LAS u32*)(lds + L_FLAG + t * 4) = 0u;
      BAR();
      {
        const u32x4 sv = {SENT, SENT, SENT, SENT};
        const int pwo_ = WS_PW + ((2 * cs + (l >> 3)) * 32 + hh) * 128 + (l & 7) * 16;
        ST128(sv, rws, WS_PO + (((4 * g + w) * 8 + (l >> 3)) * 32 + s) * 128 + (l & 7) * 16, 0, AUX_SC1);
        const int o2 = w < 2 ? (ffn ? WS_PD + ((8 * w + (l >> 3)) * 224 + uj) * 128 + (l & 7) * 16 : OOB) : (w == 2 ? (l < 16 ? pwo_ : OOB) : (red && l < 8 ? WS_X1 + rid * 128 + l * 16 : OOB));
        ST128(sv, rws, o2, 0, AUX_SC1);
      }
      SB();
      tag = __builtin_amdgcn_readfirstlane(ep) + 1u;
      float xn[4];
      if (qjob) {
        float xs[8];
#pragma unroll
        for (int i = 0; i < 4; ++i) { xs[2 * i] = bfl(xv[i]); xs[2 * i + 1] = bfh(xv[i]); }
        const float c = __uint_as_float(__builtin_amdgcn_readfirstlane(__float_as_uint(xs[0])));
        float s1 = 0.f, s2 = 0.f;
#pragma unroll
        for (int i = 0; i < 8; ++i) { const float d = xs[i] - c; s1 += d; s2 = fmaf(d, d, s2); }
        const float ss = wave_sum2(s1, s2);
        float m, den;
        ln_fin(c, __uint_as_float(__builtin_amdgcn_readlane(__float_as_uint(ss), 0)), __uint_as_float(__builtin_amdgcn_readlane(__float_as_uint(ss), 32)), m, den);
        xn[0] = rbf(rbf(bfl(xo[0]) - m) * den); xn[1] = rbf(rbf(bfh(xo[0]) - m) * den);
        xn[2] = rbf(rbf(bfl(xo[1]) - m) * den); xn[3] = rbf(rbf(bfh(xo[1]) - m) * den);
#pragma unroll
        for (int i = 0; i < 4; ++i) PIN(xn[i]);
      } else {
#pragma unroll
        for (int i = 0; i < 4; ++i) { hp[i] = pk2(silu(bfl(hp[i])), silu(bfh(hp[i]))); PIN(hp[i]); }
      }
      u32 wp[32];
#pragma unroll
      for (int ip = 0; ip < 4; ++ip)
#pragma unroll
        for (int j = 0; j < 4; ++j) { wp[8 * ip + 2 * j] = PAIR_LO(wv[2 * ip + 1][j], wv[2 * ip][j]); wp[8 * ip + 2 * j + 1] = PAIR_HI(wv[2 * ip + 1][j], wv[2 * ip][j]); }
      if (qjob) {
#pragma unroll
        for (int i = 0; i < 32; ++i) PIN(wp[i]);
        const int vo = WS_MG + (l >> 5) * 2048 + (l & 31) * 16, so = w * 512;
        POLL(PH, 1, TAGS(P[0]) != 0u && !(ABL & 16), vo, so)
        STAMPW(2, 0);
        const auto e0 = __builtin_amdgcn_permlane32_swap(P[0][0], P[0][0], false, false), e1 = __builtin_amdgcn_permlane32_swap(P[0][2], P[0][2], false, false);
        const u32 h01 = pk2(rbf(xn[0] * rbf(1.f + bfl(e0[1]))) + bfl(e0[0]), rbf(xn[1] * rbf(1.f + bfh(e0[1]))) + bfh(e0[0]));
        const u32 h23 = pk2(rbf(xn[2] * rbf(1.f + bfl(e1[1]))) + bfl(e1[0]), rbf(xn[3] * rbf(1.f + bfh(e1[1]))) + bfh(e1[0]));
        *(LAS u32x2*)(lds + L_H + w * 256 + (l & 31) * 8) = u32x2{h01, h23};
#pragma unroll
        for (int i = 0; i < 4; ++i) hp[i] = *(LAS u32*)(lds + L_H + w * 256 + i * 64 + (l >> 2) * 4);
      }
      float r0, r1;
      gemv32(wp, hp, r0, r1);
      if (lc < 4) *(LAS f32x2*)(lds + L_FM + w * 128 + lc * 32 + lg * 8) = f32x2{r0, r1};
      if (!qjob) VM0();
      LGKM0();
      u32 old = 0;
      if (l == 0) old = lds_inc(lds + L_FLAG + F_CQ);
      arr = __builtin_amdgcn_readfirstlane(old);
      if (arr == 3u) {
        const int ro = L_FM + (l & 7) * 16;
        const f32x4 v = (*(LAS f32x4*)(lds + ro) + *(LAS f32x4*)(lds + ro + 128)) + (*(LAS f32x4*)(lds + ro + 256) + *(LAS f32x4*)(lds + ro + 384));
        ST128((u32x4{pk2(v[0], v[1]), tag, pk2(v[2], v[3]), tag}), rws, l < 8 && s >= 4 ? (qjob ? WS_QG : WS_MG) + job * 128 + l * 16 : OOB, 0, AUX_SC1);
        ST32(tag, rws, l == 0 ? WS_RDY + b * 4 : OOB, 0, AUX_SC1);
        if (qjob) STAMP(3); else if (mjob && job < 32) STAMP(1);
        if (!qjob) {
#pragma nounroll
          for (int i = 0; i < QG_DELAY; ++i) __builtin_amdgcn_s_sleep(16);
        }
        const int vo = WS_QG + g * 2048 + l * 16;
        POLL(PQ, 2, (TAGS(P[0]) | TAGS(P[1])) != 0u && !(ABL & 32), vo)
        *(LAS u32x2*)(lds + L_Q + l * 8) = u32x2{P[0][0], P[0][2]};
        *(LAS u32x2*)(lds + L_Q + 512 + l * 8) = u32x2{P[1][0], P[1][2]};
        LGKM0();
        *(volatile LAS u32*)(lds + L_FLAG + F_QRDY) = 1u;
        STAMP(4);
#pragma nounroll
        for (int sp = 0; sp < SPIN_CAP; ++sp) {
          MEMF();
          const u32x4 rd = LD128(rws, WS_RDY + l * 16, 0, AUX_SC1);
          if (((rd[0] ^ tag) | (rd[1] ^ tag) | (rd[2] ^ tag) | (rd[3] ^ tag)) == 0u) break;
          __builtin_amdgcn_s_sleep(4);
        }
        *(volatile LAS u32*)(lds + L_FLAG + F_RDY) = 1u;
        STAMP(19);
      }
    }
    u32 warm = 0;
    if (arr < 2u && !(ABL & 128)) {
      const rsrc_t rwx = mk(arr == 0u ? gwg : gwu, 512 * FF * 2);
      lds_wait_ge(lds + L_FLAG + F_WARM, 4u);
      warm = LD32(rwx, (16 * s + (l >> 2)) * (FF * 2) + (l & 3) * 128, (448 * g) & ~127, AUX_WARM);
    }
    SB();
    const int kA = 4 * l + w;
    float sbA, sbB, cbA, cbB, gbv;
    f32x4 ga4, x4;
    {
      const int mo = WS_MG + 6144 + (kA >> 1) * 8;
      u32x2 m2, m3, m4, m5, m6;
      u32x4 m0;
      const u32x2 xr = LD64(rx, rid * 64 + (l & 7) * 8, 0, 0);
      lds_wait_ge(lds + L_FLAG + F_QRDY, 1u);
#pragma nounroll
      for (int sp = 0; sp < SPIN_CAP; ++sp) {
        MEMF();
        m0 = LD128(rws, WS_MG + 4096 + rid * 128 + (l & 7) * 16, 0, AUX_SC1);
        m2 = LD64(rws, mo, 0, AUX_SC1); m3 = LD64(rws, mo + 1024, 0, AUX_SC1); m4 = LD64(rws, mo + 2048, 0, AUX_SC1); m5 = LD64(rws, mo + 3072, 0, AUX_SC1);
        m6 = LD64(rws, WS_MG + 10240 + ((fin & 31) >> 1) * 128 + (lc >> 2) * 32 + (fin & 1) * 16 + (lc & 2) * 4, 0, AUX_SC1);
        if ((TAGS(m0) | (m2[1] ^ tag) | (m3[1] ^ tag) | (m4[1] ^ tag) | (m5[1] ^ tag) | (m6[1] ^ tag)) == 0u) break;
        __builtin_amdgcn_s_sleep(4);
      }
      const bool hi = w & 1;
      sbA = hi ? bfh(m2[0]) : bfl(m2[0]); sbB = hi ? bfh(m3[0]) : bfl(m3[0]);
      cbA = rbf(1.f + (hi ? bfh(m4[0]) : bfl(m4[0]))); cbB = rbf(1.f + (hi ? bfh(m5[0]) : bfl(m5[0])));
      gbv = lc & 1 ? bfh(m6[0]) : bfl(m6[0]);
      ga4 = f32x4{bfl(m0[0]), bfh(m0[0]), bfl(m0[2]), bfh(m0[2])}; x4 = f32x4{bfl(xr[0]), bfh(xr[0]), bfl(xr[1]), bfh(xr[1])};
      PIN(sbA); PIN(sbB); PIN(cbA); PIN(cbB); PIN(gbv); PIN(ga4); PIN(x4);
    }
    const u32 hm = (lc & 1) ? 0xffff0000u : 0x0000ffffu;
    const int dw = (lc >> 1) & 3;
    u32 k0 = dw == 0 ? hm : 0u, k1 = dw == 1 ? hm : 0u, k2 = dw == 2 ? hm : 0u, k3 = dw == 3 ? hm : 0u;
    int p1m = l < 32 ? WS_PO + hh * 32768 + l * 128 + 64 : OOB, p1o = WS_PO + (hh * 8 + 2 * w) * 4096 + (l >> 2) * 128 + (l & 3) * 16;
    int r1w = (lc >> 3) == (lg >> 1) ? L_R1 + (2 * w + (lg >> 1)) * 256 + (lc & 7) * 32 + (lg & 1) * 16 : L_K + t * 16;
    int pwo = l < 16 ? WS_PW + ((2 * cs + (l >> 3)) * 32 + hh) * 128 + (l & 7) * 16 : OOB, p2o = WS_PW + rid * 4096 + l * 16;
    int x1o = l < 8 ? WS_X1 + rid * 128 + l * 16 : OOB, pxo = WS_X1 + l * 16;
    PIN(x1o); PIN(pxo);
    int r2w = (l & 6) == 0 ? L_R2 + ((l >> 3) * 2 + (l & 1)) * 16 + w * 4 : L_K + t * 16;
    int pdo = lg < 2 ? WS_PD + ((4 * w + (lc >> 2)) * 224 + uj) * 128 + lg * 64 + (lc & 3) * 16 : OOB;
    int p3o = WS_PD + ((fin & 31) >> 1) * (224 * 128) + (fin & 1) * 64 + (t >> 2) * 128 + (t & 3) * 16, p3x = t < 128 ? p3o + 3 * 8192 : OOB;
    int wdo = lg * 1024 + lc * 16;
    PIN(wdo); PIN(k0); PIN(k1); PIN(k2); PIN(k3); PIN(p1m); PIN(p1o); PIN(r1w); PIN(pwo); PIN(p2o); PIN(r2w); PIN(pdo); PIN(p3o); PIN(p3x);
#define DELTA(a) __builtin_bit_cast(bf16x8, u32x4{(a) & k0, (a) & k1, (a) & k2, (a) & k3})
    lds_wait_ge(lds + L_FLAG + F_ISS, 8u);
    u32x4 wov[4], wdv[2], wgA, wgB, wuA, wuB;
    {
      const rsrc_t rwo = mk(gwo, 4096 * 512 * 2);
#pragma unroll
      for (int n = 0; n < 4; ++n) wov[n] = LD128(rwo, (4 * (lc >> 3) + lg) * 1024 + (lc & 7) * 16, (hh * 128 + 32 * w + 8 * n) * 1024 + cs * 128, AUX_NT);
      SB();
      const u32 fb = ffn && !(ABL & 2) ? 512 * FF * 2 : 0;
      const rsrc_t rwg = mk(gwg, fb), rwu = mk(gwu, fb);
      wgA = LD128(rwg, kA * (FF * 2), uj * 16, AUX_NT); wgB = LD128(rwg, kA * (FF * 2), uj * 16 + 256 * FF * 2, AUX_NT);
      wuA = LD128(rwu, kA * (FF * 2), uj * 16, AUX_NT); wuB = LD128(rwu, kA * (FF * 2), uj * 16 + 256 * FF * 2, AUX_NT);
    }
    asm volatile("" :: "v"(warm));
    SB();
    STAMPW(22, 0);
    u32 gp[16];
#pragma unroll
    for (int j = 0; j < 4; ++j) {
      gp[2 * j] = PAIR_LO(wgB[j], wgA[j]); gp[2 * j + 1] = PAIR_HI(wgB[j], wgA[j]); gp[8 + 2 * j] = PAIR_LO(wuB[j], wuA[j]); gp[8 + 2 * j + 1] = PAIR_HI(wuB[j], wuA[j]);
    }
#pragma unroll
    for (int j = 0; j < 16; ++j) PIN(gp[j]);
    lds_wait_ge(lds + L_FLAG + F_RDY, 1u);
    lds_wait_ge(lds + L_FLAG + F_ATT, 8u);
    STAMPW(15, 0);
    {
      if (w == 0) { POLL(PM, 1, max(P[4][0], P[4][1]) == SENT, p1m) }
      STAMPW(17, 0);
      __builtin_amdgcn_s_barrier();
      P1_ISSUE(p1o);
      if (w == 0) {
        const float ms = l < 32 ? __uint_as_float(P[4][0]) : -INFINITY;
        float M = fmx(ms, dpp<XOR1>(ms));
        M = fmx(M, dpp<XOR2>(M)); M = fmx(M, dpp<ROR8>(M)); M = fmx(M, dpp<ROR4>(M)); M = sw16_max(M);
        const float e = __builtin_amdgcn_exp2f((ms - M) * L2E);
        float L = e * __uint_as_float(P[4][1]);
        L += dpp<XOR1>(L); L += dpp<XOR2>(L); L += dpp<ROR8>(L); L += dpp<ROR4>(L); L = sw16_add(L, L);
        if (l < 32) *(LAS float*)(lds + L_ST + l * 4) = e * rcp(L);
      }
      BAR();
      const float e0 = *(LAS float*)(lds + L_ST + (l >> 2) * 4), e1 = *(LAS float*)(lds + L_ST + 64 + (l >> 2) * 4);
#pragma nounroll
      for (int sp = 0; sp < SPIN_CAP; ++sp) {
        P1_WAIT(0);
        const bool pend = max(max(max4(P[0]), max4(P[1])), max(max4(P[2]), max4(P[3]))) == SENT;
        if (!__any(pend)) break;
        if (pend) P1_ISSUE(p1o);
      }
      STAMPW(9, 0);
      const f32x4 va = e0 * asf(P[0]) + e1 * asf(P[1]), vb = e0 * asf(P[2]) + e1 * asf(P[3]);
      float ya = sw16_add(sw32_add(va[0], va[2]), sw32_add(va[1], va[3])), yb = sw16_add(sw32_add(vb[0], vb[2]), sw32_add(vb[1], vb[3]));
      ya += dpp<ROR8>(ya); yb += dpp<ROR8>(yb); ya += dpp<ROR4>(ya); yb += dpp<ROR4>(yb);
      const u32 y2 = pk2(ya, yb);
      const u32 n0 = lc & 8 ? dppu<0x55>(y2) : dppu<0x00>(y2), n1 = lc & 8 ? dppu<0xFF>(y2) : dppu<0xAA>(y2);
      f32x4 d0 = {0.f, 0.f, 0.f, 0.f}, d1 = d0;
      d0 = __builtin_amdgcn_mfma_f32_16x16x32_bf16(DELTA(PAIR_LO(n0, n0)), __builtin_bit_cast(bf16x8, wov[0]), d0, 0, 0, 0);
      d1 = __builtin_amdgcn_mfma_f32_16x16x32_bf16(DELTA(PAIR_LO(n1, n1)), __builtin_bit_cast(bf16x8, wov[1]), d1, 0, 0, 0);
      d0 = __builtin_amdgcn_mfma_f32_16x16x32_bf16(DELTA(PAIR_HI(n0, n0)), __builtin_bit_cast(bf16x8, wov[2]), d0, 0, 0, 0);
      d1 = __builtin_amdgcn_mfma_f32_16x16x32_bf16(DELTA(PAIR_HI(n1, n1)), __builtin_bit_cast(bf16x8, wov[3]), d1, 0, 0, 0);
      *(LAS f32x4*)(lds + r1w) = d0 + d1;
      BAR();
      if (w == 0) {
        const f32x4 r = *(LAS f32x4*)(lds + L_R1 + (2 * lg) * 256 + lc * 16) + *(LAS f32x4*)(lds + L_R1 + (2 * lg + 1) * 256 + lc * 16);
        f32x4 q;
#pragma unroll
        for (int i = 0; i < 4; ++i) q[i] = sw16_add(r[i], r[i]);
#pragma unroll
        for (int i = 0; i < 4; ++i) q[i] = sw32_add(q[i], q[i]);
        ST128(canon4(q), rws, pwo, 0, AUX_SC1);
      }
      STAMPW(10, 0);
    }
    if (red && w == 1) {
      f32x4 v;
      P2_SUMS(v, p2o)
#pragma unroll
      for (int i = 0; i < 4; ++i) v[i] = sw32_add(v[i], v[i]);
#pragma unroll
      for (int i = 0; i < 4; ++i) v[i] = sw16_add(v[i], v[i]);
#pragma unroll
      for (int i = 0; i < 4; ++i) { v[i] += dpp<ROR8>(v[i]); v[i] = rbf(x4[i] + rbf(ga4[i] * rbf(v[i]))); }
      ST128(canon4(v), rws, x1o, 0, AUX_SC1);
      STAMP(18);
    }
    if (ffn) {
      {
        const rsrc_t rwd = mk(gwd, 512 * FF * 2);
        asm volatile("s_nop 4\n\tbuffer_load_dwordx4 %0, %2, %3, %4 offen nt\n\tbuffer_load_dwordx4 %1, %2, %3, %5 offen nt"
                     : "=&v"(wdv[0]), "=&v"(wdv[1]) : "v"(wdo), "s"(rwd), "s"(uj * 8192 + w * 256), "s"(uj * 8192 + 4096 + w * 256) : "memory");
      }
      if (w == 0) {
        POLL(PX, 2, max(max4(P[0]), max4(P[1])) == SENT, pxo)
        *(LAS u32x4*)(lds + L_XS + l * 16) = P[0]; *(LAS u32x4*)(lds + L_XS + 1024 + l * 16) = P[1];
      } else VM0();
      asm volatile("" : "+v"(wdv[0]), "+v"(wdv[1]));
      STAMPW(11, 0);
      BAR();
      const f32x4 xa = *(LAS f32x4*)(lds + L_XS + l * 16), xb = *(LAS f32x4*)(lds + L_XS + 1024 + l * 16);
      const float x1A = *(LAS float*)(lds + L_XS + kA * 4), x1B = *(LAS float*)(lds + L_XS + 1024 + kA * 4);
      const float cw = *(LAS float*)(lds + L_XS);
      const f32x4 da = xa - cw, db = xb - cw, d1 = da + db, d2 = da * da + db * db;
      const float ss = wave_sum2((d1[0] + d1[1]) + (d1[2] + d1[3]), (d2[0] + d2[1]) + (d2[2] + d2[3]));
      float m, den;
      ln_fin(cw, __uint_as_float(__builtin_amdgcn_readlane(__float_as_uint(ss), 0)), __uint_as_float(__builtin_amdgcn_readlane(__float_as_uint(ss), 32)), m, den);
      const u32 hp = pk2(rbf(rbf(rbf(x1A - m) * den) * cbA) + sbA, rbf(rbf(rbf(x1B - m) * den) * cbB) + sbB);
      float a[16];
#pragma unroll
      for (int j = 0; j < 16; ++j) a[j] = dot2(gp[j], hp, 0.f);
#pragma unroll
      for (int j = 0; j < 8; ++j) a[j] = sw32_add(a[j], a[j + 8]);
#pragma unroll
      for (int j = 0; j < 4; ++j) a[j] = sw16_add(a[j], a[j + 4]);
#pragma unroll
      for (int j = 0; j < 2; ++j) { const float lo = a[j] + dpp<ROR8>(a[j]), hi = a[j + 2] + dpp<ROR8>(a[j + 2]); a[j] = l & 8 ? hi : lo; }
      { const float lo = a[0] + dpp<XOR1>(a[0]), hi = a[1] + dpp<XOR1>(a[1]); a[0] = l & 1 ? hi : lo; }
      a[0] += dpp<XOR2>(a[0]);
      a[0] += dpp<0x141>(dpp<0x1B>(a[0]));
      *(LAS float*)(lds + r2w) = a[0];
      BAR();
      {
        float act[2];
#pragma unroll
        for (int n = 0; n < 2; ++n) {
          const int un = 4 * n + lg;
          const f32x4 gs = *(LAS f32x4*)(lds + L_R2 + un * 16), us = *(LAS f32x4*)(lds + L_R2 + (8 + un) * 16);
          const float gg = rbf((gs[0] + gs[1]) + (gs[2] + gs[3])), uu = rbf((us[0] + us[1]) + (us[2] + us[3]));
          const float z = gg * fmaf(gg * gg, -2.f * L2E * 0.7978845608028654f * 0.044715f, -2.f * L2E * 0.7978845608028654f);
          act[n] = rbf(gg * rcp(1.f + __builtin_amdgcn_exp2f(z))) * uu;
        }
        const u32 av = pk2(act[0], act[1]);
        f32x4 d = {0.f, 0.f, 0.f, 0.f};
        d = __builtin_amdgcn_mfma_f32_16x16x32_bf16(DELTA(PAIR_LO(av, av)), __builtin_bit_cast(bf16x8, wdv[0]), d, 0, 0, 0);
        d = __builtin_amdgcn_mfma_f32_16x16x32_bf16(DELTA(PAIR_HI(av, av)), __builtin_bit_cast(bf16x8, wdv[1]), d, 0, 0, 0);
        ST128(canon4(d), rws, pdo, 0, AUX_SC1);
      }
      STAMPW(12, 0);
      if (s < 4) {
        const int oc = 32 * (fin >> 1) + 8 * (lc >> 2) + 4 * (fin & 1) + (lc & 3);
        const float xo = *(LAS float*)(lds + L_XS + oc * 4);
        f32x4 v;
        P3_SUMS(v, p3o, p3x)
        STAMPW(13, 0);
        float y = sw16_add(sw32_add(v[0], v[2]), sw32_add(v[1], v[3]));
        y += dpp<ROR8>(y); y += dpp<ROR4>(y);
        if (lc < 4) *(LAS float*)(lds + L_R3 + (4 * lc + lg) * 16 + w * 4) = y;
        BAR();
        if (t < 16) {
          const f32x4 rs = *(LAS f32x4*)(lds + L_R3 + t * 16);
          gout[oc] = __builtin_bit_cast(u16, (__bf16)(xo + rbf(gbv * rbf((rs[0] + rs[1]) + (rs[2] + rs[3])))));
          if (b == 0 && t == 0) ST32(tag, rws, WS_EPOCH, 0, AUX_SC1);
        }
        STAMPW(14, 0);
      }
    }
    KEEP();
  }
}

extern "C" void kernel_launch(void* const* d_in, const int* in_sizes, int n_in, void* d_out, int out_size, void* d_ws, size_t ws_size, hipStream_t stream) {
  (void)hipFuncSetAttribute((const void*)fused_block, hipFuncAttributeMaxDynamicSharedMemorySize, L_END);
  fused_block<<<256, 768, L_END, stream>>>((const u16*)d_in[0], (const u16*)d_in[1], (const u16*)d_in[2], (const u16*)d_in[3], (const u16*)d_in[4],
                                           (const u16*)d_in[6], (const u16*)d_in[7], (const u16*)d_in[8], (const u16*)d_in[9], (const u16*)d_in[10],
                                           (u16*)d_out, (unsigned char*)d_ws);
}
```

```cpp
#include <hip/hip_runtime.h>
#include <hip/hip_bf16.h>

typedef unsigned short u16;
typedef unsigned int u32;
typedef unsigned long long u64;
typedef u32 u32x2 __attribute__((ext_vector_type(2)));
typedef u32 u32x4 __attribute__((ext_vector_type(4)));
typedef float f32x2 __attribute__((ext_vector_type(2)));
typedef float f32x4 __attribute__((ext_vector_type(4)));
typedef short s16x4 __attribute__((ext_vector_type(4)));
typedef __bf16 bf16x2 __attribute__((ext_vector_type(2)));
typedef __bf16 bf16x8 __attribute__((ext_vector_type(8)));
typedef __amdgpu_buffer_rsrc_t rsrc_t;
#define LAS __attribute__((address_space(3)))

#define STAMP(k) do {} while (0)
#define STAMPW(k, wv) do {} while (0)
#define DBGCNT(k, v) do {} while (0)
#define ABL 0
#define WIN 4
#define WLO 2
#define GO_MODE 1
#define QPOS 12
#define WARM_AT 6
#define POLL_GAP 1
#define POLL_SLOW 12
#define POLL_HI 160
#define P1_HI 24
#define AUX_WARM 0
#define QG_DELAY 4

constexpr int D = 512, SK = 16384, DH = 128, FF = 1792;
constexpr u32 SENT = 0xFFFFFFFFu;
constexpr int AUX_NT = 2, AUX_SC1 = 16;
constexpr int SPIN_CAP = 1 << 16;
constexpr int WS_EPOCH = 0;
constexpr int WS_RDY = 1024;
constexpr int WS_MG = 4096;
constexpr int WS_QG = WS_MG + 1536 * 8;
constexpr int WS_X1 = WS_QG + 16384;
constexpr int WS_PW = WS_X1 + 2048;
constexpr int WS_PO = WS_PW + 65536;
constexpr int WS_PD = WS_PO + 1048576;
constexpr int WS_END = WS_PD + 16 * 224 * 128;
constexpr int OOB = 0x40000000;
constexpr int L_K = 0;
constexpr int L_ACC = 131072;
constexpr int L_Q = L_ACC + 16384;
constexpr int L_H = L_Q + 1024;
constexpr int L_M = L_H + 1024;
constexpr int L_L = L_M + 128;
constexpr int L_R1 = L_L + 128;
constexpr int L_XS = L_R1 + 2048;
constexpr int L_ST = L_XS + 2048;
constexpr int L_R2 = L_ST + 128;
constexpr int L_R3 = L_R2 + 256;
constexpr int L_FM = L_R3 + 256;
constexpr int L_FLAG = L_FM + 512;
constexpr int F_CQ = 0, F_QRDY = 4, F_RDY = 8, F_ISS = 12, F_ATT = 16, F_WARM = 20, F_SM = 24, F_GO = 28;
constexpr int L_STAMP = L_FLAG + 64;
constexpr int L_END = L_STAMP + 256;
constexpr float L2E = 1.4426950408889634f;

__device__ __forceinline__ float bfl(u32 x) { return __uint_as_float(x << 16); }
__device__ __forceinline__ float bfh(u32 x) { return __uint_as_float(x & 0xffff0000u); }
__device__ __forceinline__ u32 pk2(float lo, float hi) { bf16x2 v = {(__bf16)lo, (__bf16)hi}; return __builtin_bit_cast(u32, v); }
__device__ __forceinline__ float rbf(float f) { return (float)(__bf16)f; }
__device__ __forceinline__ float fmx(float a, float b) { return __builtin_elementwise_maximum(a, b); }
template <int C> __device__ __forceinline__ u32 dppu(u32 x) { return (u32)__builtin_amdgcn_mov_dpp((int)x, C, 0xf, 0xf, true); }
template <int C> __device__ __forceinline__ float dpp(float x) { return __uint_as_float(dppu<C>(__float_as_uint(x))); }
constexpr int XOR1 = 0xB1, XOR2 = 0x4E, ROR4 = 0x124, ROR8 = 0x128;
__device__ __forceinline__ float sw16_add(float a, float b) {
  auto r = __builtin_amdgcn_permlane16_swap(__float_as_uint(a), __float_as_uint(b), false, false);
  return __uint_as_float(r[0]) + __uint_as_float(r[1]);
}
__device__ __forceinline__ float sw32_add(float a, float b) {
  auto r = __builtin_amdgcn_permlane32_swap(__float_as_uint(a), __float_as_uint(b), false, false);
  return __uint_as_float(r[0]) + __uint_as_float(r[1]);
}
__device__ __forceinline__ float sw16_max(float a) {
  auto r = __builtin_amdgcn_permlane16_swap(__float_as_uint(a), __float_as_uint(a), false, false);
  return fmx(__uint_as_float(r[0]), __uint_as_float(r[1]));
}
__device__ __forceinline__ float sw32_max(float a) {
  auto r = __builtin_amdgcn_permlane32_swap(__float_as_uint(a), __float_as_uint(a), false, false);
  return fmx(__uint_as_float(r[0]), __uint_as_float(r[1]));
}
__device__ __forceinline__ float wave_sum2(float a, float b) {
  float x = sw32_add(a, b);
  x = sw16_add(x, x);
  x += dpp<XOR1>(x); x += dpp<XOR2>(x); x += dpp<ROR8>(x); x += dpp<ROR4>(x);
  return x;
}
__device__ __forceinline__ rsrc_t mk(const void* p, u32 bytes) { return __builtin_amdgcn_make_buffer_rsrc((void*)p, 0, bytes, 0x00020000); }
#define LD128(r, v, s, a) __builtin_amdgcn_raw_buffer_load_b128(r, v, s, a)
#define LD64(r, v, s, a) __builtin_amdgcn_raw_buffer_load_b64(r, v, s, a)
#define LD32(r, v, s, a) __builtin_amdgcn_raw_buffer_load_b32(r, v, s, a)
#define ST128(d, r, v, s, a) __builtin_amdgcn_raw_buffer_store_b128(d, r, v, s, a)
#define ST64(d, r, v, s, a) __builtin_amdgcn_raw_buffer_store_b64(d, r, v, s, a)
#define ST32(d, r, v, s, a) __builtin_amdgcn_raw_buffer_store_b32(d, r, v, s, a)
__device__ __forceinline__ u32 canon(float f) { return min(__float_as_uint(f), 0xFFFFFFFEu); }
__device__ __forceinline__ u32x4 canon4(f32x4 v) { return u32x4{canon(v[0]), canon(v[1]), canon(v[2]), canon(v[3])}; }
__device__ __forceinline__ u32 max4(u32x4 v) { return max(max(v[0], v[1]), max(v[2], v[3])); }
__device__ __forceinline__ f32x4 asf(u32x4 v) { return __builtin_bit_cast(f32x4, v); }
__device__ __forceinline__ float rcp(float x) { return __builtin_amdgcn_rcpf(x); }
__device__ __forceinline__ float silu(float x) { return x * rcp(1.f + __expf(-x)); }
__device__ __forceinline__ float dot2(u32 a, u32 b, float c) { return __builtin_amdgcn_fdot2_f32_bf16(__builtin_bit_cast(bf16x2, a), __builtin_bit_cast(bf16x2, b), c, false); }
#define MEMF() asm volatile("" ::: "memory")
#define LGKM0() asm volatile("s_waitcnt lgkmcnt(0)" ::: "memory")
#define VM0() asm volatile("s_waitcnt vmcnt(0)" ::: "memory")
#define BAR() do { LGKM0(); __builtin_amdgcn_s_barrier(); MEMF(); } while (0)
#define SB() __builtin_amdgcn_sched_barrier(0)
#define PACE() asm volatile("s_waitcnt vmcnt(%0)" :: "i"(WIN - 1) : "memory")
#define PIN(x) asm volatile("" : "+v"(x))
__device__ __forceinline__ u32 lds_inc(LAS unsigned char* p) { return __hip_atomic_fetch_add((LAS u32*)p, 1u, __ATOMIC_RELAXED, __HIP_MEMORY_SCOPE_WORKGROUP); }
__device__ __forceinline__ void lds_wait_ge(LAS unsigned char* p, u32 n) {
#pragma nounroll
  for (int sp = 0; sp < SPIN_CAP * 16; ++sp) { if (*(volatile LAS u32*)p >= n) break; __builtin_amdgcn_s_sleep(1); }
  MEMF();
}
__device__ __forceinline__ void ln_fin(float c, float S1, float S2, float& m, float& den) {
  const float d = S1 * (1.f / D);
  const float var = rbf(fmaxf(fmaf(-d, d, S2 * (1.f / D)), 0.f));
  m = rbf(c + d); den = rcp(rbf(__builtin_amdgcn_sqrtf(rbf(var + 1e-5f))));
}
__device__ __forceinline__ void gemv32(const u32 (&wp)[32], const u32 (&hp)[4], float& r0, float& r1) {
  float a[8];
#pragma unroll
  for (int j = 0; j < 8; ++j) a[j] = dot2(wp[24 + j], hp[3], dot2(wp[16 + j], hp[2], dot2(wp[8 + j], hp[1], dot2(wp[j], hp[0], 0.f))));
#pragma unroll
  for (int j = 0; j < 4; ++j) a[j] = sw32_add(a[j], a[j + 4]);
#pragma unroll
  for (int j = 0; j < 2; ++j) a[j] = sw16_add(a[j], a[j + 2]);
#pragma unroll
  for (int j = 0; j < 2; ++j) { a[j] += dpp<ROR8>(a[j]); a[j] += dpp<ROR4>(a[j]); }
  r0 = a[0]; r1 = a[1];
}
#define PAIR_LO(hi, lo) __builtin_amdgcn_perm(hi, lo, 0x05040100u)
#define PAIR_HI(hi, lo) __builtin_amdgcn_perm(hi, lo, 0x07060302u)
#define PH_ISSUE(vo_, so_) asm volatile("s_nop 4\n\t" \
               "buffer_load_dwordx4 v[100:103], %[vo], %[rs], %[so] offen sc1\n\t" \
                : "+{v[100:103]}"(P[0]) : [vo] "v"(vo_), [so] "s"(so_), [rs] "s"(rws) : "memory")
#define PH_WAIT(n) asm volatile("s_waitcnt vmcnt(" #n ")" : "+{v[100:103]}"(P[0]) :: "memory")
#define PH_TIE() asm volatile("" : "+{v[100:103]}"(P[0]))
#define PQ_ISSUE(vo_) asm volatile("s_nop 4\n\t" \
               "buffer_load_dwordx4 v[100:103], %[vo], %[rs], 0 offen sc1\n\t" \
               "buffer_load_dwordx4 v[104:107], %[vo], %[rs], 0 offen offset:1024 sc1\n\t" \
                : "+{v[100:103]}"(P[0]), "+{v[104:107]}"(P[1]) : [vo] "v"(vo_), [rs] "s"(rws) : "memory")
#define PQ_WAIT(n) asm volatile("s_waitcnt vmcnt(" #n ")" : "+{v[100:103]}"(P[0]), "+{v[104:107]}"(P[1]) :: "memory")
#define PQ_TIE() asm volatile("" : "+{v[100:103]}"(P[0]), "+{v[104:107]}"(P[1]))
#define PM_ISSUE(vm_) asm volatile("s_nop 4\n\t" \
               "buffer_load_dwordx4 v[116:119], %[vm], %[rs], 0 offen sc1\n\t" \
                : "+{v[116:119]}"(P[4]) : [vm] "v"(vm_), [rs] "s"(rws) : "memory")
#define PM_WAIT(n) asm volatile("s_waitcnt vmcnt(" #n ")" : "+{v[116:119]}"(P[4]) :: "memory")
#define PM_TIE() asm volatile("" : "+{v[116:119]}"(P[4]))
#define P1_ISSUE(vo_) asm volatile("s_nop 4\n\t" \
               "buffer_load_dwordx4 v[100:103], %[vo], %[rs], 0 offen sc1\n\t" \
               "buffer_load_dwordx4 v[104:107], %[vo], %[rs], 0 offen offset:2048 sc1\n\t" \
               "buffer_load_dwordx4 v[108:111], %[vo], %[rs], %[s1] offen sc1\n\t" \
               "buffer_load_dwordx4 v[112:115], %[vo], %[rs], %[s1] offen offset:2048 sc1\n\t" \
                : "+{v[100:103]}"(P[0]), "+{v[104:107]}"(P[1]), "+{v[108:111]}"(P[2]), "+{v[112:115]}"(P[3]) : [vo] "v"(vo_), [s1] "s"(4096), [rs] "s"(rws) : "memory")
#define P1_WAIT(n) asm volatile("s_waitcnt vmcnt(" #n ")" : "+{v[100:103]}"(P[0]), "+{v[104:107]}"(P[1]), "+{v[108:111]}"(P[2]), "+{v[112:115]}"(P[3]) :: "memory")
#define P1_TIE() asm volatile("" : "+{v[100:103]}"(P[0]), "+{v[104:107]}"(P[1]), "+{v[108:111]}"(P[2]), "+{v[112:115]}"(P[3]))
#define PX_ISSUE(vo_) asm volatile("s_nop 4\n\t" \
               "buffer_load_dwordx4 v[100:103], %[vo], %[rs], 0 offen sc1\n\t" \
               "buffer_load_dwordx4 v[104:107], %[vo], %[rs], 0 offen offset:1024 sc1\n\t" \
                : "+{v[100:103]}"(P[0]), "+{v[104:107]}"(P[1]) : [vo] "v"(vo_), [rs] "s"(rws) : "memory")
#define PX_WAIT(n) asm volatile("s_waitcnt vmcnt(" #n ")" : "+{v[100:103]}"(P[0]), "+{v[104:107]}"(P[1]) :: "memory")
#define PX_TIE() asm volatile("" : "+{v[100:103]}"(P[0]), "+{v[104:107]}"(P[1]))
#define P2_SUMS(r0_, vo_) { int cnt_ = SPIN_CAP / 4, np_, tt_; f32x4 t0_, t1_; asm volatile( \
      "s_nop 4\n\t" \
      "4:\n\t" \
      "buffer_load_dwordx4 v[100:103], %[vo], %[rs], 0 offen sc1\n\t" \
      "buffer_load_dwordx4 v[104:107], %[vo], %[rs], 0 offen offset:1024 sc1\n\t" \
      "buffer_load_dwordx4 v[108:111], %[vo], %[rs], 0 offen offset:2048 sc1\n\t" \
      "buffer_load_dwordx4 v[112:115], %[vo], %[rs], 0 offen offset:3072 sc1\n\t" \
      "1:\n\t" \
      "s_waitcnt vmcnt(0)\n\t" \
      "v_pk_add_f32 v[92:93], v[100:101], v[104:105]\n\t" \
      "v_pk_add_f32 v[94:95], v[102:103], v[106:107]\n\t" \
      "v_pk_add_f32 v[84:85], v[108:109], v[112:113]\n\t" \
      "v_pk_add_f32 v[86:87], v[110:111], v[114:115]\n\t" \
      "v_pk_add_f32 v[92:93], v[92:93], v[84:85]\n\t" \
      "v_pk_add_f32 v[94:95], v[94:95], v[86:87]\n\t" \
      "s_nop 0\n\t" \
      "v_pk_add_f32 v[84:85], v[92:93], v[94:95]\n\t" \
      "s_nop 0\n\t" \
      "v_cmp_u_f32 vcc, v84, v85\n\t" \
      "s_cbranch_vccz 2f\n\t" \
      "s_sub_u32 %[cnt], %[cnt], 1\n\t" \
      "s_cbranch_scc1 2f\n\t" \
      "s_mov_b32 %[np], 0\n\t" \
      "v_cmp_eq_u32 vcc, -1, v100\n\t" \
      "s_bcnt1_i32_b64 %[t], vcc\n\t" \
      "s_add_u32 %[np], %[np], %[t]\n\t" \
      "v_cmp_eq_u32 vcc, -1, v104\n\t" \
      "s_bcnt1_i32_b64 %[t], vcc\n\t" \
      "s_add_u32 %[np], %[np], %[t]\n\t" \
      "v_cmp_eq_u32 vcc, -1, v108\n\t" \
      "s_bcnt1_i32_b64 %[t], vcc\n\t" \
      "s_add_u32 %[np], %[np], %[t]\n\t" \
      "v_cmp_eq_u32 vcc, -1, v112\n\t" \
      "s_bcnt1_i32_b64 %[t], vcc\n\t" \
      "s_add_u32 %[np], %[np], %[t]\n\t" \
      "s_cmp_eq_u32 %[np], 0\n\t" \
      "s_cbranch_scc1 4b\n\t" \
      "s_cmp_gt_u32 %[np], %[hi]\n\t" \
      "s_cbranch_scc0 3f\n\t" \
      "s_sleep %[slow]\n\t" \
      "3:\n\t" \
      "s_sleep %[gap]\n\t" \
      "v_cmp_eq_u32 vcc, -1, v100\n\t" \
      "s_mov_b64 exec, vcc\n\t" \
      "s_cbranch_execz 5f\n\t" \
      "buffer_load_dwordx4 v[100:103], %[vo], %[rs], 0 offen sc1\n\t" \
      "5:\n\t" \
      "s_mov_b64 exec, -1\n\t" \
      "v_cmp_eq_u32 vcc, -1, v104\n\t" \
      "s_mov_b64 exec, vcc\n\t" \
      "s_cbranch_execz 5f\n\t" \
      "buffer_load_dwordx4 v[104:107], %[vo], %[rs], 0 offen offset:1024 sc1\n\t" \
      "5:\n\t" \
      "s_mov_b64 exec, -1\n\t" \
      "v_cmp_eq_u32 vcc, -1, v108\n\t" \
      "s_mov_b64 exec, vcc\n\t" \
      "s_cbranch_execz 5f\n\t" \
      "buffer_load_dwordx4 v[108:111], %[vo], %[rs], 0 offen offset:2048 sc1\n\t" \
      "5:\n\t" \
      "s_mov_b64 exec, -1\n\t" \
      "v_cmp_eq_u32 vcc, -1, v112\n\t" \
      "s_mov_b64 exec, vcc\n\t" \
      "s_cbranch_execz 5f\n\t" \
      "buffer_load_dwordx4 v[112:115], %[vo], %[rs], 0 offen offset:3072 sc1\n\t" \
      "5:\n\t" \
      "s_mov_b64 exec, -1\n\t" \
      "s_branch 1b\n\t" \
      "2:\n\t" "s_nop 0" \
      : "+{v[100:103]}"(P[0]), "+{v[104:107]}"(P[1]), "+{v[108:111]}"(P[2]), "+{v[112:115]}"(P[3]), "=&{v[92:95]}"(r0_), "=&{v[84:87]}"(t0_), "=&{v[88:91]}"(t1_), [cnt] "+&s"(cnt_), [np] "=&s"(np_), [t] "=&s"(tt_) \
      : [vo] "v"(vo_), [rs] "s"(rws), [gap] "i"(POLL_GAP), [slow] "i"(POLL_SLOW), [hi] "i"(POLL_HI) : "vcc", "scc", "memory"); DBGCNT(21, SPIN_CAP / 4 - cnt_ + 1); }
#define P3_SUMS(r0_, vo_, v3_) { int cnt_ = SPIN_CAP / 4, np_, tt_; f32x4 t0_, t1_; asm volatile( \
      "s_nop 4\n\t" \
      "4:\n\t" \
      "buffer_load_dwordx4 v[100:103], %[vo], %[rs], 0 offen sc1\n\t" \
      "buffer_load_dwordx4 v[104:107], %[vo], %[rs], %[s1] offen sc1\n\t" \
      "buffer_load_dwordx4 v[108:111], %[vo], %[rs], %[s2] offen sc1\n\t" \
      "buffer_load_dwordx4 v[112:115], %[v3], %[rs], 0 offen sc1\n\t" \
      "1:\n\t" \
      "s_waitcnt vmcnt(0)\n\t" \
      "v_pk_add_f32 v[92:93], v[100:101], v[104:105]\n\t" \
      "v_pk_add_f32 v[94:95], v[102:103], v[106:107]\n\t" \
      "v_pk_add_f32 v[84:85], v[108:109], v[112:113]\n\t" \
      "v_pk_add_f32 v[86:87], v[110:111], v[114:115]\n\t" \
      "v_pk_add_f32 v[92:93], v[92:93], v[84:85]\n\t" \
      "v_pk_add_f32 v[94:95], v[94:95], v[86:87]\n\t" \
      "s_nop 0\n\t" \
      "v_pk_add_f32 v[84:85], v[92:93], v[94:95]\n\t" \
      "s_nop 0\n\t" \
      "v_cmp_u_f32 vcc, v84, v85\n\t" \
      "s_cbranch_vccz 2f\n\t" \
      "s_sub_u32 %[cnt], %[cnt], 1\n\t" \
      "s_cbranch_scc1 2f\n\t" \
      "s_mov_b32 %[np], 0\n\t" \
      "v_cmp_eq_u32 vcc, -1, v100\n\t" \
      "s_bcnt1_i32_b64 %[t], vcc\n\t" \
      "s_add_u32 %[np], %[np], %[t]\n\t" \
      "v_cmp_eq_u32 vcc, -1, v104\n\t" \
      "s_bcnt1_i32_b64 %[t], vcc\n\t" \
      "s_add_u32 %[np], %[np], %[t]\n\t" \
      "v_cmp_eq_u32 vcc, -1, v108\n\t" \
      "s_bcnt1_i32_b64 %[t], vcc\n\t" \
      "s_add_u32 %[np], %[np], %[t]\n\t" \
      "v_cmp_eq_u32 vcc, -1, v112\n\t" \
      "s_bcnt1_i32_b64 %[t], vcc\n\t" \
      "s_add_u32 %[np], %[np], %[t]\n\t" \
      "s_cmp_eq_u32 %[np], 0\n\t" \
      "s_cbranch_scc1 4b\n\t" \
      "s_cmp_gt_u32 %[np], %[hi]\n\t" \
      "s_cbranch_scc0 3f\n\t" \
      "s_sleep %[slow]\n\t" \
      "3:\n\t" \
      "s_sleep %[gap]\n\t" \
      "v_cmp_eq_u32 vcc, -1, v100\n\t" \
      "s_mov_b64 exec, vcc\n\t" \
      "s_cbranch_execz 5f\n\t" \
      "buffer_load_dwordx4 v[100:103], %[vo], %[rs], 0 offen sc1\n\t" \
      "5:\n\t" \
      "s_mov_b64 exec, -1\n\t" \
      "v_cmp_eq_u32 vcc, -1, v104\n\t" \
      "s_mov_b64 exec, vcc\n\t" \
      "s_cbranch_execz 5f\n\t" \
      "buffer_load_dwordx4 v[104:107], %[vo], %[rs], %[s1] offen sc1\n\t" \
      "5:\n\t" \
      "s_mov_b64 exec, -1\n\t" \
      "v_cmp_eq_u32 vcc, -1, v108\n\t" \
      "s_mov_b64 exec, vcc\n\t" \
      "s_cbranch_execz 5f\n\t" \
      "buffer_load_dwordx4 v[108:111], %[vo], %[rs], %[s2] offen sc1\n\t" \
      "5:\n\t" \
      "s_mov_b64 exec, -1\n\t" \
      "v_cmp_eq_u32 vcc, -1, v112\n\t" \
      "s_mov_b64 exec, vcc\n\t" \
      "s_cbranch_execz 5f\n\t" \
      "buffer_load_dwordx4 v[112:115], %[v3], %[rs], 0 offen sc1\n\t" \
      "5:\n\t" \
      "s_mov_b64 exec, -1\n\t" \
      "s_branch 1b\n\t" \
      "2:\n\t" "s_nop 0" \
      : "+{v[100:103]}"(P[0]), "+{v[104:107]}"(P[1]), "+{v[108:111]}"(P[2]), "+{v[112:115]}"(P[3]), "=&{v[92:95]}"(r0_), "=&{v[84:87]}"(t0_), "=&{v[88:91]}"(t1_), [cnt] "+&s"(cnt_), [np] "=&s"(np_), [t] "=&s"(tt_) \
      : [vo] "v"(vo_), [v3] "v"(v3_), [s1] "s"(8192), [s2] "s"(16384), [rs] "s"(rws), [gap] "i"(POLL_GAP), [slow] "i"(POLL_SLOW), [hi] "i"(POLL_HI) : "vcc", "scc", "memory"); DBGCNT(16, SPIN_CAP / 4 - cnt_ + 1); }
#define KEEP() asm volatile("; keep" :: "{v[100:103]}"(P[0]), "{v[104:107]}"(P[1]), "{v[108:111]}"(P[2]), "{v[112:115]}"(P[3]), "{v[116:119]}"(P[4]), "{v[120:123]}"(P[5]))
#define POOL_INIT() asm volatile("; pool" : "={v[100:103]}"(P[0]), "={v[104:107]}"(P[1]), "={v[108:111]}"(P[2]), "={v[112:115]}"(P[3]), "={v[116:119]}"(P[4]), "={v[120:123]}"(P[5]))

#define POLL(NAME, N, PEND, ...)                     \
  NAME##_ISSUE(__VA_ARGS__); NAME##_WAIT(0);                                                      \
  { bool pend_ = PEND;                                                                            \
    if (__any(pend_)) {                                                                           \
      if (pend_) NAME##_ISSUE(__VA_ARGS__);                                                       \
      __builtin_amdgcn_s_sleep(4);                                                                \
      _Pragma("nounroll") for (int sp = 0; sp < SPIN_CAP; ++sp) {                                 \
        if (pend_) NAME##_ISSUE(__VA_ARGS__);                                                     \
        NAME##_WAIT(N); pend_ = PEND;                                                             \
        if (!__any(pend_)) break;                                                                 \
      } }                                                                                         \
    NAME##_TIE(); }
#define POLLB(NAME, HI, PEND, ...)     \
  NAME##_ISSUE(__VA_ARGS__);                                                                      \
  _Pragma("nounroll") for (int sp = 0; sp < SPIN_CAP; ++sp) {                                     \
    NAME##_WAIT(0);                                                                               \
    DBGCNT(20, sp + 1);                                                                           \
    const bool pend_ = PEND;                                                                      \
    const u64 bal_ = __ballot(pend_);                                                             \
    if (bal_ == 0) break;                                                                         \
    if (__popcll(bal_) > HI) __builtin_amdgcn_s_sleep(POLL_SLOW);                                 \
    __builtin_amdgcn_s_sleep(POLL_GAP);                                                           \
    if (pend_) NAME##_ISSUE(__VA_ARGS__);                                                         \
  }

__global__ void __launch_bounds__(768, 3)
fused_block(const u16* __restrict__ gx, const u16* __restrict__ gk, const u16* __restrict__ gv, const u16* __restrict__ gwq,
            const u16* __restrict__ gwo, const u16* __restrict__ gwg, const u16* __restrict__ gwu, const u16* __restrict__ gwd,
            const u16* __restrict__ gc, const u16* __restrict__ gwm, u16* __restrict__ gout, unsigned char* __restrict__ gws) {
  extern __shared__ __attribute__((aligned(16))) unsigned char smem[];
  LAS unsigned char* lds = (LAS unsigned char*)smem;
  const int b = blockIdx.x, g = b & 7, s = b >> 3;
  const int t = threadIdx.x, w = __builtin_amdgcn_readfirstlane(t >> 6), l = t & 63, lg = l >> 4, lc = l & 15;
  const rsrc_t rws = mk(gws, WS_END);
  STAMPW(0, 0);
  asm volatile("" :: "s"(gx), "s"(gk), "s"(gv), "s"(gwq), "s"(gwo), "s"(gwg), "s"(gwu), "s"(gwd), "s"(gc), "s"(gwm), "s"(gout), "s"(gws));
  if (__builtin_expect(w >= 4, 1)) {
    const int a = w - 4;
    const size_t kvo = ((size_t)g * SK + s * 512) * DH;
    const rsrc_t rk = mk(gk + kvo, (ABL & 256) ? 0 : 131072), rv = mk(gv + kvo, (ABL & 256) ? 0 : 131072);
    const bool thin0 = GO_MODE == 1 || s >= 16 || (s >= 4 && 12 * g + (s - 4) < 32);
    bool thin = thin0;
#define PACE_DYN() do { if (thin) { asm volatile("s_waitcnt vmcnt(%0)" :: "i"(WLO - 1) : "memory");                                  \
                                    thin = __builtin_amdgcn_readfirstlane(*(volatile LAS u32*)(lds + L_FLAG + F_GO)) == 0u; } else PACE(); } while (0)
    {
      asm volatile("s_nop 4" ::: "memory");
      const int v0 = lg * 256 + ((lc ^ (4 * lg + 0)) * 16), v1 = lg * 256 + ((lc ^ (4 * lg + 1)) * 16);
      const int v2 = lg * 256 + ((lc ^ (4 * lg + 2)) * 16), v3 = lg * 256 + ((lc ^ (4 * lg + 3)) * 16);
      const int vk[4] = {v0, v1, v2, v3};
      if (thin) __builtin_amdgcn_s_barrier();
#pragma unroll
      for (int i = 0; i < 16; ++i) {
        const int so = a * 16384 + i * 1024;
        if (i == 2 && !thin0) __builtin_amdgcn_s_barrier();
        PACE_DYN();
        asm volatile("s_mov_b32 m0, %2\n\ts_nop 0\n\tbuffer_load_dwordx4 %0, %1, %2 offen nt lds" :: "v"(vk[i & 3]), "s"(rk), "s"(so) : "memory");
      }
    }
    u32x4 vf[16];
#define VLOAD(i) vf[i] = LD128(rv, l * 16 + ((i) & 3) * 1024, a * 16384 + ((i) >> 2) * 4096, AUX_NT)
#pragma unroll
    for (int i = 0; i < QPOS; ++i) { PACE_DYN(); VLOAD(i); if (i == WARM_AT && l == 0) lds_inc(lds + L_FLAG + F_WARM); }
    SB();
    const u32 hm = (lc & 1) ? 0xffff0000u : 0x0000ffffu;
    u32 m0 = (lc & 2) ? 0u : hm, m1 = (lc & 2) ? hm : 0u;
    int ka[4], pubo = WS_PO + (((4 * g + (a & 3)) * 8 + (l >> 3)) * 32 + s) * 128 + (l & 7) * 16;
#pragma unroll
    for (int tt = 0; tt < 4; ++tt) { ka[tt] = L_K + a * 16384 + (4 * (lc & 3) + (lc >> 2)) * 256 + (((4 * tt + lg) ^ lc) * 16); PIN(ka[tt]); }
    PIN(m0); PIN(m1); PIN(pubo);
    STAMPW(5, 4);
    lds_wait_ge(lds + L_FLAG + F_QRDY, 1u);
#pragma unroll
    for (int i = QPOS; i < 16; ++i) VLOAD(i);
    SB();
    if (l == 0) lds_inc(lds + L_FLAG + F_ISS);
    u32x4 qf[4];
#pragma unroll
    for (int tt = 0; tt < 4; ++tt) qf[tt] = *(LAS u32x4*)(lds + L_Q + (lc >> 2) * 256 + tt * 64 + lg * 16);
    asm volatile("s_waitcnt vmcnt(16)" ::: "memory");
    STAMPW(6, 4);
    f32x4 sc;
    {
      f32x4 c[4];
#pragma unroll
      for (int T = 0; T < 4; ++T) {
        c[T] = f32x4{0.f, 0.f, 0.f, 0.f};
#pragma unroll
        for (int tt = 0; tt < 4; ++tt)
          c[T] = __builtin_amdgcn_mfma_f32_16x16x32_bf16(__builtin_bit_cast(bf16x8, *(LAS u32x4*)(lds + ka[tt] + T * 4096)), __builtin_bit_cast(bf16x8, qf[tt]), c[T], 0, 0, 0);
      }
      const f32x4 lo = (l & 1) ? c[1] : c[0], hi = (l & 1) ? c[3] : c[2];
      sc = (l & 2) ? hi : lo;
    }
#pragma unroll
    for (int r = 0; r < 4; ++r) sc[r] = rbf(rbf(sc[r]) * 0.08838834764831845f);
    float mw = fmx(fmx(sc[0], sc[1]), fmx(sc[2], sc[3]));
    mw = fmx(mw, dpp<XOR1>(mw)); mw = fmx(mw, dpp<XOR2>(mw)); mw = sw32_max(sw16_max(mw));
    u32 pp[4];
    float lw = 0.f;
#pragma unroll
    for (int r = 0; r < 4; ++r) { const float p = __builtin_amdgcn_exp2f((sc[r] - mw) * L2E); lw += p; pp[r] = pk2(p, p); }
    f32x4 o0 = {0.f, 0.f, 0.f, 0.f}, o1 = {0.f, 0.f, 0.f, 0.f};
#define PV(i, Q) { const u32 x_ = dppu<Q>(pp[(i) & 3]); const u32x2 pa_ = {x_ & m0, x_ & m1};                                                         \
      o0 = __builtin_amdgcn_mfma_f32_16x16x16bf16_1k(__builtin_bit_cast(s16x4, pa_), __builtin_bit_cast(s16x4, u32x2{vf[i][0], vf[i][1]}), o0, 0, 0, 0); \
      o1 = __builtin_amdgcn_mfma_f32_16x16x16bf16_1k(__builtin_bit_cast(s16x4, pa_), __builtin_bit_cast(s16x4, u32x2{vf[i][2], vf[i][3]}), o1, 0, 0, 0); }
    PV(0, 0x00) PV(1, 0x00) PV(2, 0x00) PV(3, 0x00)
    lw += dpp<XOR1>(lw); lw += dpp<XOR2>(lw); lw = sw16_add(lw, lw); lw = sw32_add(lw, lw);
    if (lg == 0 && (lc & 3) == 0) { *(LAS float*)(lds + L_M + (lc >> 2) * 32 + a * 4) = mw; *(LAS float*)(lds + L_L + (lc >> 2) * 32 + a * 4) = lw; }
    LGKM0();
    if (l == 0) lds_inc(lds + L_FLAG + F_SM);
    PV(4, 0x55) PV(5, 0x55) PV(6, 0x55) PV(7, 0x55) PV(8, 0xAA) PV(9, 0xAA) PV(10, 0xAA) PV(11, 0xAA)
    float e[8], M = 0.f, L = 0.f;
    if (a < 4) {
      lds_wait_ge(lds + L_FLAG + F_SM, 8u);
      const f32x4 ma = *(LAS f32x4*)(lds + L_M + a * 32), mb = *(LAS f32x4*)(lds + L_M + a * 32 + 16);
      const f32x4 la = *(LAS f32x4*)(lds + L_L + a * 32), lb = *(LAS f32x4*)(lds + L_L + a * 32 + 16);
      M = fmx(fmx(fmx(ma[0], ma[1]), fmx(ma[2], ma[3])), fmx(fmx(mb[0], mb[1]), fmx(mb[2], mb[3])));
#pragma unroll
      for (int i = 0; i < 8; ++i) {
        e[i] = __builtin_amdgcn_exp2f(((i < 4 ? ma[i & 3] : mb[i & 3]) - M) * L2E);
        L = fmaf(e[i], i < 4 ? la[i & 3] : lb[i & 3], L);
        PIN(e[i]);
      }
      PIN(L);
    }
    SB();
    PV(12, 0xFF) PV(13, 0xFF) PV(14, 0xFF) PV(15, 0xFF)
    __builtin_amdgcn_s_setprio(3);
    *(LAS f32x4*)(lds + L_ACC + a * 2048 + lg * 512 + lc * 32) = o0;
    *(LAS f32x4*)(lds + L_ACC + a * 2048 + lg * 512 + lc * 32 + 16) = o1;
    LGKM0();
    if (l == 0) lds_inc(lds + L_FLAG + F_ATT);
    STAMPW(7, 11); STAMPW(23, 4);
    if (a < 4) {
      const int ao = L_ACC + a * 512 + ((l >> 3) * 4 + (l & 3)) * 16;
      lds_wait_ge(lds + L_FLAG + F_ATT, 8u);
      f32x4 oa = e[0] * *(LAS f32x4*)(lds + ao), ob = e[1] * *(LAS f32x4*)(lds + ao + 2048);
#pragma unroll
      for (int i = 2; i < 8; i += 2) { oa += e[i] * *(LAS f32x4*)(lds + ao + i * 2048); ob += e[i + 1] * *(LAS f32x4*)(lds + ao + (i + 1) * 2048); }
      f32x4 o = oa + ob;
      if (l & 4) o = f32x4{M, L, 0.f, 0.f};
      ST128(canon4(o), rws, pubo, 0, AUX_SC1);
      STAMPW(8, 4);
    }
  } else {
    const rsrc_t rx = mk(gx, 1024);
    const int hh = 4 * g + (s >> 3), cs = s & 7, uj = 28 * g + s, fin = 4 * g + s;
    const bool ffn = s < 28, red = s == 28 || s == 29;
    const int rid = 2 * g + (s & 1);
    u32 tag, arr;
    u32x4 P[6];
    POOL_INIT();
#define TAGS(v) ((v[1] ^ tag) | (v[3] ^ tag))
    {
      const bool qjob = s >= 16, mjob = s >= 4 && s < 16;
      const int job = qjob ? 16 * g + (s - 16) : 12 * g + (s - 4);
      const int pitch = qjob ? 8192 : 6144;
      const rsrc_t rc = mk(gc, 1024), rw = mk(qjob ? gwq : gwm, qjob ? ((ABL & 8) ? 0 : 512 * 8192) : (mjob && !(ABL & 4) ? 512 * 6144 : 0));
      u32x4 wv[8];
#pragma unroll
      for (int i = 0; i < 8; ++i) wv[i] = LD128(rw, (l >> 2) * 2 * pitch + (l & 3) * 16 + job * 64, (128 * w + (i >> 1) * 32 + (i & 1)) * pitch, AUX_NT);
      SB();
      u32 hp[4];
#pragma unroll
      for (int i = 0; i < 4; ++i) hp[i] = LD32(rc, (l >> 2) * 4 + i * 64, w * 256, 0);
      const u32 ep = LD32(rws, WS_EPOCH, 0, AUX_SC1);
      const u32x4 xv = LD128(rx, l * 16, 0, 0);
      const u32x2 xo = LD64(rx, (l & 31) * 8, w * 256, 0);
      SB();
      if (t < 8) *(volatile LAS u32*)(lds + L_FLAG + t * 4) = 0u;
      BAR();
      {
        const u32x4 sv = {SENT, SENT, SENT, SENT};
        const int pwo_ = WS_PW + ((2 * cs + (l >> 3)) * 32 + hh) * 128 + (l & 7) * 16;
        ST128(sv, rws, WS_PO + (((4 * g + w) * 8 + (l >> 3)) * 32 + s) * 128 + (l & 7) * 16, 0, AUX_SC1);
        const int o2 = w < 2 ? (ffn ? WS_PD + ((8 * w + (l >> 3)) * 224 + uj) * 128 + (l & 7) * 16 : OOB) : (w == 2 ? (l < 16 ? pwo_ : OOB) : (red && l < 8 ? WS_X1 + rid * 128 + l * 16 : OOB));
        ST128(sv, rws, o2, 0, AUX_SC1);
      }
      SB();
      tag = __builtin_amdgcn_readfirstlane(ep) + 1u;
      float xn[4];
      if (qjob) {
        float xs[8];
#pragma unroll
        for (int i = 0; i < 4; ++i) { xs[2 * i] = bfl(xv[i]); xs[2 * i + 1] = bfh(xv[i]); }
        const float c = __uint_as_float(__builtin_amdgcn_readfirstlane(__float_as_uint(xs[0])));
        float s1 = 0.f, s2 = 0.f;
#pragma unroll
        for (int i = 0; i < 8; ++i) { const float d = xs[i] - c; s1 += d; s2 = fmaf(d, d, s2); }
        const float ss = wave_sum2(s1, s2);
        float m, den;
        ln_fin(c, __uint_as_float(__builtin_amdgcn_readlane(__float_as_uint(ss), 0)), __uint_as_float(__builtin_amdgcn_readlane(__float_as_uint(ss), 32)), m, den);
        xn[0] = rbf(rbf(bfl(xo[0]) - m) * den); xn[1] = rbf(rbf(bfh(xo[0]) - m) * den);
        xn[2] = rbf(rbf(bfl(xo[1]) - m) * den); xn[3] = rbf(rbf(bfh(xo[1]) - m) * den);
#pragma unroll
        for (int i = 0; i < 4; ++i) PIN(xn[i]);
      } else {
#pragma unroll
        for (int i = 0; i < 4; ++i) { hp[i] = pk2(silu(bfl(hp[i])), silu(bfh(hp[i]))); PIN(hp[i]); }
      }
      u32 wp[32];
#pragma unroll
      for (int ip = 0; ip < 4; ++ip)
#pragma unroll
        for (int j = 0; j < 4; ++j) { wp[8 * ip + 2 * j] = PAIR_LO(wv[2 * ip + 1][j], wv[2 * ip][j]); wp[8 * ip + 2 * j + 1] = PAIR_HI(wv[2 * ip + 1][j], wv[2 * ip][j]); }
      if (qjob) {
#pragma unroll
        for (int i = 0; i < 32; ++i) PIN(wp[i]);
        const int vo = WS_MG + (l >> 5) * 2048 + (l & 31) * 16, so = w * 512;
        POLL(PH, 1, TAGS(P[0]) != 0u && !(ABL & 16), vo, so)
        STAMPW(2, 0);
        const auto e0 = __builtin_amdgcn_permlane32_swap(P[0][0], P[0][0], false, false), e1 = __builtin_amdgcn_permlane32_swap(P[0][2], P[0][2], false, false);
        const u32 h01 = pk2(rbf(xn[0] * rbf(1.f + bfl(e0[1]))) + bfl(e0[0]), rbf(xn[1] * rbf(1.f + bfh(e0[1]))) + bfh(e0[0]));
        const u32 h23 = pk2(rbf(xn[2] * rbf(1.f + bfl(e1[1]))) + bfl(e1[0]), rbf(xn[3] * rbf(1.f + bfh(e1[1]))) + bfh(e1[0]));
        *(LAS u32x2*)(lds + L_H + w * 256 + (l & 31) * 8) = u32x2{h01, h23};
#pragma unroll
        for (int i = 0; i < 4; ++i) hp[i] = *(LAS u32*)(lds + L_H + w * 256 + i * 64 + (l >> 2) * 4);
      }
      float r0, r1;
      gemv32(wp, hp, r0, r1);
      if (lc < 4) *(LAS f32x2*)(lds + L_FM + w * 128 + lc * 32 + lg * 8) = f32x2{r0, r1};
      if (!qjob) VM0();
      LGKM0();
      u32 old = 0;
      if (l == 0) old = lds_inc(lds + L_FLAG + F_CQ);
      arr = __builtin_amdgcn_readfirstlane(old);
      if (arr == 3u) {
        const int ro = L_FM + (l & 7) * 16;
        const f32x4 v = (*(LAS f32x4*)(lds + ro) + *(LAS f32x4*)(lds + ro + 128)) + (*(LAS f32x4*)(lds + ro + 256) + *(LAS f32x4*)(lds + ro + 384));
        ST128((u32x4{pk2(v[0], v[1]), tag, pk2(v[2], v[3]), tag}), rws, l < 8 && s >= 4 ? (qjob ? WS_QG : WS_MG) + job * 128 + l * 16 : OOB, 0, AUX_SC1);
        ST32(tag, rws, l == 0 ? WS_RDY + b * 4 : OOB, 0, AUX_SC1);
        if (GO_MODE == 0) *(volatile LAS u32*)(lds + L_FLAG + F_GO) = 1u;
        if (qjob) STAMP(3); else if (mjob && job < 32) STAMP(1);
        if (!qjob) {
#pragma nounroll
          for (int i = 0; i < QG_DELAY; ++i) __builtin_amdgcn_s_sleep(16);
        }
        const int vo = WS_QG + g * 2048 + l * 16;
        POLL(PQ, 2, (TAGS(P[0]) | TAGS(P[1])) != 0u && !(ABL & 32), vo)
        *(LAS u32x2*)(lds + L_Q + l * 8) = u32x2{P[0][0], P[0][2]};
        *(LAS u32x2*)(lds + L_Q + 512 + l * 8) = u32x2{P[1][0], P[1][2]};
        LGKM0();
        *(volatile LAS u32*)(lds + L_FLAG + F_QRDY) = 1u;
        if (GO_MODE == 1) *(volatile LAS u32*)(lds + L_FLAG + F_GO) = 1u;
        STAMP(4);
#pragma nounroll
        for (int sp = 0; sp < SPIN_CAP; ++sp) {
          MEMF();
          const u32x4 rd = LD128(rws, WS_RDY + l * 16, 0, AUX_SC1);
          if (((rd[0] ^ tag) | (rd[1] ^ tag) | (rd[2] ^ tag) | (rd[3] ^ tag)) == 0u) break;
          __builtin_amdgcn_s_sleep(4);
        }
        *(volatile LAS u32*)(lds + L_FLAG + F_RDY) = 1u;
        STAMP(19);
      }
    }
    u32 warm = 0;
    if (arr < 2u && !(ABL & 128)) {
      const rsrc_t rwx = mk(arr == 0u ? gwg : gwu, 512 * FF * 2);
      lds_wait_ge(lds + L_FLAG + F_WARM, 4u);
      warm = LD32(rwx, (16 * s + (l >> 2)) * (FF * 2) + (l & 3) * 128, (448 * g) & ~127, AUX_WARM);
    }
    SB();
    const int kA = 4 * l + w;
    float sbA, sbB, cbA, cbB, gbv;
    f32x4 ga4, x4;
    {
      const int mo = WS_MG + 6144 + (kA >> 1) * 8;
      u32x2 m2, m3, m4, m5, m6;
      u32x4 m0;
      const u32x2 xr = LD64(rx, rid * 64 + (l & 7) * 8, 0, 0);
      lds_wait_ge(lds + L_FLAG + F_QRDY, 1u);
#pragma nounroll
      for (int sp = 0; sp < SPIN_CAP; ++sp) {
        MEMF();
        m0 = LD128(rws, WS_MG + 4096 + rid * 128 + (l & 7) * 16, 0, AUX_SC1);
        m2 = LD64(rws, mo, 0, AUX_SC1); m3 = LD64(rws, mo + 1024, 0, AUX_SC1); m4 = LD64(rws, mo + 2048, 0, AUX_SC1); m5 = LD64(rws, mo + 3072, 0, AUX_SC1);
        m6 = LD64(rws, WS_MG + 10240 + ((fin & 31) >> 1) * 128 + (lc >> 2) * 32 + (fin & 1) * 16 + (lc & 2) * 4, 0, AUX_SC1);
        if ((TAGS(m0) | (m2[1] ^ tag) | (m3[1] ^ tag) | (m4[1] ^ tag) | (m5[1] ^ tag) | (m6[1] ^ tag)) == 0u) break;
        __builtin_amdgcn_s_sleep(4);
      }
      const bool hi = w & 1;
      sbA = hi ? bfh(m2[0]) : bfl(m2[0]); sbB = hi ? bfh(m3[0]) : bfl(m3[0]);
      cbA = rbf(1.f + (hi ? bfh(m4[0]) : bfl(m4[0]))); cbB = rbf(1.f + (hi ? bfh(m5[0]) : bfl(m5[0])));
      gbv = lc & 1 ? bfh(m6[0]) : bfl(m6[0]);
      ga4 = f32x4{bfl(m0[0]), bfh(m0[0]), bfl(m0[2]), bfh(m0[2])}; x4 = f32x4{bfl(xr[0]), bfh(xr[0]), bfl(xr[1]), bfh(xr[1])};
      PIN(sbA); PIN(sbB); PIN(cbA); PIN(cbB); PIN(gbv); PIN(ga4); PIN(x4);
    }
    const u32 hm = (lc & 1) ? 0xffff0000u : 0x0000ffffu;
    const int dw = (lc >> 1) & 3;
    u32 k0 = dw == 0 ? hm : 0u, k1 = dw == 1 ? hm : 0u, k2 = dw == 2 ? hm : 0u, k3 = dw == 3 ? hm : 0u;
    int p1m = l < 32 ? WS_PO + hh * 32768 + l * 128 + 64 : OOB, p1o = WS_PO + (hh * 8 + 2 * w) * 4096 + (l >> 2) * 128 + (l & 3) * 16;
    int r1w = (lc >> 3) == (lg >> 1) ? L_R1 + (2 * w + (lg >> 1)) * 256 + (lc & 7) * 32 + (lg & 1) * 16 : L_K + t * 16;
    int pwo = l < 16 ? WS_PW + ((2 * cs + (l >> 3)) * 32 + hh) * 128 + (l & 7) * 16 : OOB, p2o = WS_PW + rid * 4096 + l * 16;
    int x1o = l < 8 ? WS_X1 + rid * 128 + l * 16 : OOB, pxo = WS_X1 + l * 16;
    PIN(x1o); PIN(pxo);
    int r2w = (l & 6) == 0 ? L_R2 + ((l >> 3) * 2 + (l & 1)) * 16 + w * 4 : L_K + t * 16;
    int pdo = lg < 2 ? WS_PD + ((4 * w + (lc >> 2)) * 224 + uj) * 128 + lg * 64 + (lc & 3) * 16 : OOB;
    int p3o = WS_PD + ((fin & 31) >> 1) * (224 * 128) + (fin & 1) * 64 + (t >> 2) * 128 + (t & 3) * 16, p3x = t < 128 ? p3o + 3 * 8192 : OOB;
    int wdo = lg * 1024 + lc * 16;
    PIN(wdo); PIN(k0); PIN(k1); PIN(k2); PIN(k3); PIN(p1m); PIN(p1o); PIN(r1w); PIN(pwo); PIN(p2o); PIN(r2w); PIN(pdo); PIN(p3o); PIN(p3x);
#define DELTA(a) __builtin_bit_cast(bf16x8, u32x4{(a) & k0, (a) & k1, (a) & k2, (a) & k3})
    lds_wait_ge(lds + L_FLAG + F_ISS, 8u);
    u32x4 wov[4], wdv[2], wgA, wgB, wuA, wuB;
    {
      const rsrc_t rwo = mk(gwo, 4096 * 512 * 2);
#pragma unroll
      for (int n = 0; n < 4; ++n) wov[n] = LD128(rwo, (4 * (lc >> 3) + lg) * 1024 + (lc & 7) * 16, (hh * 128 + 32 * w + 8 * n) * 1024 + cs * 128, AUX_NT);
      SB();
      const u32 fb = ffn && !(ABL & 2) ? 512 * FF * 2 : 0;
      const rsrc_t rwg = mk(gwg, fb), rwu = mk(gwu, fb);
      wgA = LD128(rwg, kA * (FF * 2), uj * 16, AUX_NT); wgB = LD128(rwg, kA * (FF * 2), uj * 16 + 256 * FF * 2, AUX_NT);
      wuA = LD128(rwu, kA * (FF * 2), uj * 16, AUX_NT); wuB = LD128(rwu, kA * (FF * 2), uj * 16 + 256 * FF * 2, AUX_NT);
    }
    asm volatile("" :: "v"(warm));
    SB();
    STAMPW(22, 0);
    u32 gp[16];
#pragma unroll
    for (int j = 0; j < 4; ++j) {
      gp[2 * j] = PAIR_LO(wgB[j], wgA[j]); gp[2 * j + 1] = PAIR_HI(wgB[j], wgA[j]); gp[8 + 2 * j] = PAIR_LO(wuB[j], wuA[j]); gp[8 + 2 * j + 1] = PAIR_HI(wuB[j], wuA[j]);
    }
#pragma unroll
    for (int j = 0; j < 16; ++j) PIN(gp[j]);
    lds_wait_ge(lds + L_FLAG + F_RDY, 1u);
    lds_wait_ge(lds + L_FLAG + F_ATT, 8u);
    STAMPW(15, 0);
    {
      if (w == 0) { POLL(PM, 1, max(P[4][0], P[4][1]) == SENT, p1m) }
      STAMPW(17, 0);
      __builtin_amdgcn_s_barrier();
      P1_ISSUE(p1o);
      if (w == 0) {
        const float ms = l < 32 ? __uint_as_float(P[4][0]) : -INFINITY;
        float M = fmx(ms, dpp<XOR1>(ms));
        M = fmx(M, dpp<XOR2>(M)); M = fmx(M, dpp<ROR8>(M)); M = fmx(M, dpp<ROR4>(M)); M = sw16_max(M);
        const float e = __builtin_amdgcn_exp2f((ms - M) * L2E);
        float L = e * __uint_as_float(P[4][1]);
        L += dpp<XOR1>(L); L += dpp<XOR2>(L); L += dpp<ROR8>(L); L += dpp<ROR4>(L); L = sw16_add(L, L);
        if (l < 32) *(LAS float*)(lds + L_ST + l * 4) = e * rcp(L);
      }
      BAR();
      const float e0 = *(LAS float*)(lds + L_ST + (l >> 2) * 4), e1 = *(LAS float*)(lds + L_ST + 64 + (l >> 2) * 4);
#pragma nounroll
      for (int sp = 0; sp < SPIN_CAP; ++sp) {
        P1_WAIT(0);
        const bool pend = max(max(max4(P[0]), max4(P[1])), max(max4(P[2]), max4(P[3]))) == SENT;
        if (!__any(pend)) break;
        if (pend) P1_ISSUE(p1o);
      }
      STAMPW(9, 0);
      const f32x4 va = e0 * asf(P[0]) + e1 * asf(P[1]), vb = e0 * asf(P[2]) + e1 * asf(P[3]);
      float ya = sw16_add(sw32_add(va[0], va[2]), sw32_add(va[1], va[3])), yb = sw16_add(sw32_add(vb[0], vb[2]), sw32_add(vb[1], vb[3]));
      ya += dpp<ROR8>(ya); yb += dpp<ROR8>(yb); ya += dpp<ROR4>(ya); yb += dpp<ROR4>(yb);
      const u32 y2 = pk2(ya, yb);
      const u32 n0 = lc & 8 ? dppu<0x55>(y2) : dppu<0x00>(y2), n1 = lc & 8 ? dppu<0xFF>(y2) : dppu<0xAA>(y2);
      f32x4 d0 = {0.f, 0.f, 0.f, 0.f}, d1 = d0;
      d0 = __builtin_amdgcn_mfma_f32_16x16x32_bf16(DELTA(PAIR_LO(n0, n0)), __builtin_bit_cast(bf16x8, wov[0]), d0, 0, 0, 0);
      d1 = __builtin_amdgcn_mfma_f32_16x16x32_bf16(DELTA(PAIR_LO(n1, n1)), __builtin_bit_cast(bf16x8, wov[1]), d1, 0, 0, 0);
      d0 = __builtin_amdgcn_mfma_f32_16x16x32_bf16(DELTA(PAIR_HI(n0, n0)), __builtin_bit_cast(bf16x8, wov[2]), d0, 0, 0, 0);
      d1 = __builtin_amdgcn_mfma_f32_16x16x32_bf16(DELTA(PAIR_HI(n1, n1)), __builtin_bit_cast(bf16x8, wov[3]), d1, 0, 0, 0);
      *(LAS f32x4*)(lds + r1w) = d0 + d1;
      BAR();
      if (w == 0) {
        const f32x4 r = *(LAS f32x4*)(lds + L_R1 + (2 * lg) * 256 + lc * 16) + *(LAS f32x4*)(lds + L_R1 + (2 * lg + 1) * 256 + lc * 16);
        f32x4 q;
#pragma unroll
        for (int i = 0; i < 4; ++i) q[i] = sw16_add(r[i], r[i]);
#pragma unroll
        for (int i = 0; i < 4; ++i) q[i] = sw32_add(q[i], q[i]);
        ST128(canon4(q), rws, pwo, 0, AUX_SC1);
      }
      STAMPW(10, 0);
    }
    if (red && w == 1) {
      f32x4 v;
      P2_SUMS(v, p2o)
#pragma unroll
      for (int i = 0; i < 4; ++i) v[i] = sw32_add(v[i], v[i]);
#pragma unroll
      for (int i = 0; i < 4; ++i) v[i] = sw16_add(v[i], v[i]);
#pragma unroll
      for (int i = 0; i < 4; ++i) { v[i] += dpp<ROR8>(v[i]); v[i] = rbf(x4[i] + rbf(ga4[i] * rbf(v[i]))); }
      ST128(canon4(v), rws, x1o, 0, AUX_SC1);
      STAMP(18);
    }
    if (ffn) {
      {
        const rsrc_t rwd = mk(gwd, 512 * FF * 2);
        asm volatile("s_nop 4\n\tbuffer_load_dwordx4 %0, %2, %3, %4 offen nt\n\tbuffer_load_dwordx4 %1, %2, %3, %5 offen nt"
                     : "=&v"(wdv[0]), "=&v"(wdv[1]) : "v"(wdo), "s"(rwd), "s"(uj * 8192 + w * 256), "s"(uj * 8192 + 4096 + w * 256) : "memory");
      }
      if (w == 0) {
        POLL(PX, 2, max(max4(P[0]), max4(P[1])) == SENT, pxo)
        *(LAS u32x4*)(lds + L_XS + l * 16) = P[0]; *(LAS u32x4*)(lds + L_XS + 1024 + l * 16) = P[1];
      } else VM0();
      asm volatile("" : "+v"(wdv[0]), "+v"(wdv[1]));
      STAMPW(11, 0);
      BAR();
      const f32x4 xa = *(LAS f32x4*)(lds + L_XS + l * 16), xb = *(LAS f32x4*)(lds + L_XS + 1024 + l * 16);
      const float x1A = *(LAS float*)(lds + L_XS + kA * 4), x1B = *(LAS float*)(lds + L_XS + 1024 + kA * 4);
      const float cw = *(LAS float*)(lds + L_XS);
      const f32x4 da = xa - cw, db = xb - cw, d1 = da + db, d2 = da * da + db * db;
      const float ss = wave_sum2((d1[0] + d1[1]) + (d1[2] + d1[3]), (d2[0] + d2[1]) + (d2[2] + d2[3]));
      float m, den;
      ln_fin(cw, __uint_as_float(__builtin_amdgcn_readlane(__float_as_uint(ss), 0)), __uint_as_float(__builtin_amdgcn_readlane(__float_as_uint(ss), 32)), m, den);
      const u32 hp = pk2(rbf(rbf(rbf(x1A - m) * den) * cbA) + sbA, rbf(rbf(rbf(x1B - m) * den) * cbB) + sbB);
      float a[16];
#pragma unroll
      for (int j = 0; j < 16; ++j) a[j] = dot2(gp[j], hp, 0.f);
#pragma unroll
      for (int j = 0; j < 8; ++j) a[j] = sw32_add(a[j], a[j + 8]);
#pragma unroll
      for (int j = 0; j < 4; ++j) a[j] = sw16_add(a[j], a[j + 4]);
#pragma unroll
      for (int j = 0; j < 2; ++j) { const float lo = a[j] + dpp<ROR8>(a[j]), hi = a[j + 2] + dpp<ROR8>(a[j + 2]); a[j] = l & 8 ? hi : lo; }
      { const float lo = a[0] + dpp<XOR1>(a[0]), hi = a[1] + dpp<XOR1>(a[1]); a[0] = l & 1 ? hi : lo; }
      a[0] += dpp<XOR2>(a[0]);
      a[0] += dpp<0x141>(dpp<0x1B>(a[0]));
      *(LAS float*)(lds + r2w) = a[0];
      BAR();
      {
        float act[2];
#pragma unroll
        for (int n = 0; n < 2; ++n) {
          const int un = 4 * n + lg;
          const f32x4 gs = *(LAS f32x4*)(lds + L_R2 + un * 16), us = *(LAS f32x4*)(lds + L_R2 + (8 + un) * 16);
          const float gg = rbf((gs[0] + gs[1]) + (gs[2] + gs[3])), uu = rbf((us[0] + us[1]) + (us[2] + us[3]));
          const float z = gg * fmaf(gg * gg, -2.f * L2E * 0.7978845608028654f * 0.044715f, -2.f * L2E * 0.7978845608028654f);
          act[n] = rbf(gg * rcp(1.f + __builtin_amdgcn_exp2f(z))) * uu;
        }
        const u32 av = pk2(act[0], act[1]);
        f32x4 d = {0.f, 0.f, 0.f, 0.f};
        d = __builtin_amdgcn_mfma_f32_16x16x32_bf16(DELTA(PAIR_LO(av, av)), __builtin_bit_cast(bf16x8, wdv[0]), d, 0, 0, 0);
        d = __builtin_amdgcn_mfma_f32_16x16x32_bf16(DELTA(PAIR_HI(av, av)), __builtin_bit_cast(bf16x8, wdv[1]), d, 0, 0, 0);
        ST128(canon4(d), rws, pdo, 0, AUX_SC1);
      }
      STAMPW(12, 0);
      if (s < 4) {
        const int oc = 32 * (fin >> 1) + 8 * (lc >> 2) + 4 * (fin & 1) + (lc & 3);
        const float xo = *(LAS float*)(lds + L_XS + oc * 4);
        f32x4 v;
        P3_SUMS(v, p3o, p3x)
        STAMPW(13, 0);
        float y = sw16_add(sw32_add(v[0], v[2]), sw32_add(v[1], v[3]));
        y += dpp<ROR8>(y); y += dpp<ROR4>(y);
        if (lc < 4) *(LAS float*)(lds + L_R3 + (4 * lc + lg) * 16 + w * 4) = y;
        BAR();
        if (t < 16) {
          const f32x4 rs = *(LAS f32x4*)(lds + L_R3 + t * 16);
          gout[oc] = __builtin_bit_cast(u16, (__bf16)(xo + rbf(gbv * rbf((rs[0] + rs[1]) + (rs[2] + rs[3])))));
          if (b == 0 && t == 0) ST32(tag, rws, WS_EPOCH, 0, AUX_SC1);
        }
        STAMPW(14, 0);
      }
    }
    KEEP();
  }
}

extern "C" void kernel_launch(void* const* d_in, const int* in_sizes, int n_in, void* d_out, int out_size, void* d_ws, size_t ws_size, hipStream_t stream) {
  (void)hipFuncSetAttribute((const void*)fused_block, hipFuncAttributeMaxDynamicSharedMemorySize, L_END);
  fused_block<<<256, 768, L_END, stream>>>((const u16*)d_in[0], (const u16*)d_in[1], (const u16*)d_in[2], (const u16*)d_in[3], (const u16*)d_in[4],
                                           (const u16*)d_in[6], (const u16*)d_in[7], (const u16*)d_in[8], (const u16*)d_in[9], (const u16*)d_in[10],
                                           (u16*)d_out, (unsigned char*)d_ws);
}
```

```cpp
#include <hip/hip_runtime.h>
#include <hip/hip_bf16.h>
#include <cstdio>

typedef unsigned short u16;
typedef unsigned int u32;
typedef unsigned long long u64;
typedef u32 u32x2 __attribute__((ext_vector_type(2)));
typedef u32 u32x4 __attribute__((ext_vector_type(4)));
typedef float f32x2 __attribute__((ext_vector_type(2)));
typedef float f32x4 __attribute__((ext_vector_type(4)));
typedef short s16x4 __attribute__((ext_vector_type(4)));
typedef __bf16 bf16x2 __attribute__((ext_vector_type(2)));
typedef __bf16 bf16x8 __attribute__((ext_vector_type(8)));
typedef __amdgpu_buffer_rsrc_t rsrc_t;
#define LAS __attribute__((address_space(3)))

#define STAMP(k) do {} while (0)
#define STAMPW(k, wv) do {} while (0)
#define DBGCNT(k, v) do {} while (0)
#define ABL 0
#define WIN 4
#define QPOS 12
#define WARM_AT 9
#define POLL_GAP 1
#define POLL_SLOW 12
#define POLL_HI 160
#define P1_HI 24
#define AUX_WARM 0
#define QG_DELAY 4

constexpr int D = 512, SK = 16384, DH = 128, FF = 1792;
constexpr u32 SENT = 0xFFFFFFFFu;
constexpr int AUX_NT = 2, AUX_SC1 = 16;
constexpr int SPIN_CAP = 1 << 16;
constexpr int WS_EPOCH = 0;
constexpr int WS_RDY = 1024;
constexpr int WS_MG = 4096;
constexpr int WS_QG = WS_MG + 1536 * 8;
constexpr int WS_X1 = WS_QG + 16384;
constexpr int WS_PW = WS_X1 + 2048;
constexpr int WS_PO = WS_PW + 65536;
constexpr int WS_PD = WS_PO + 1048576;
constexpr int WS_END = WS_PD + 16 * 224 * 128;
constexpr int OOB = 0x40000000;
constexpr int L_K = 0;
constexpr int L_ACC = 131072;
constexpr int L_Q = L_ACC + 16384;
constexpr int L_H = L_Q + 1024;
constexpr int L_M = L_H + 1024;
constexpr int L_L = L_M + 128;
constexpr int L_R1 = L_L + 128;
constexpr int L_XS = L_R1 + 2048;
constexpr int L_ST = L_XS + 2048;
constexpr int L_R2 = L_ST + 128;
constexpr int L_R3 = L_R2 + 256;
constexpr int L_FM = L_R3 + 256;
constexpr int L_FLAG = L_FM + 512;
constexpr int F_CQ = 0, F_QRDY = 4, F_RDY = 8, F_ISS = 12, F_ATT = 16, F_WARM = 20, F_SM = 24;
constexpr int L_STAMP = L_FLAG + 64;
constexpr int L_END = L_STAMP + 256;
constexpr float L2E = 1.4426950408889634f;

__device__ __forceinline__ float bfl(u32 x) { return __uint_as_float(x << 16); }
__device__ __forceinline__ float bfh(u32 x) { return __uint_as_float(x & 0xffff0000u); }
__device__ __forceinline__ u32 pk2(float lo, float hi) { bf16x2 v = {(__bf16)lo, (__bf16)hi}; return __builtin_bit_cast(u32, v); }
__device__ __forceinline__ float rbf(float f) { return (float)(__bf16)f; }
__device__ __forceinline__ float fmx(float a, float b) { return __builtin_elementwise_maximum(a, b); }
template <int C> __device__ __forceinline__ u32 dppu(u32 x) { return (u32)__builtin_amdgcn_mov_dpp((int)x, C, 0xf, 0xf, true); }
template <int C> __device__ __forceinline__ float dpp(float x) { return __uint_as_float(dppu<C>(__float_as_uint(x))); }
constexpr int XOR1 = 0xB1, XOR2 = 0x4E, ROR4 = 0x124, ROR8 = 0x128;
__device__ __forceinline__ float sw16_add(float a, float b) {
  auto r = __builtin_amdgcn_permlane16_swap(__float_as_uint(a), __float_as_uint(b), false, false);
  return __uint_as_float(r[0]) + __uint_as_float(r[1]);
}
__device__ __forceinline__ float sw32_add(float a, float b) {
  auto r = __builtin_amdgcn_permlane32_swap(__float_as_uint(a), __float_as_uint(b), false, false);
  return __uint_as_float(r[0]) + __uint_as_float(r[1]);
}
__device__ __forceinline__ float sw16_max(float a) {
  auto r = __builtin_amdgcn_permlane16_swap(__float_as_uint(a), __float_as_uint(a), false, false);
  return fmx(__uint_as_float(r[0]), __uint_as_float(r[1]));
}
__device__ __forceinline__ float sw32_max(float a) {
  auto r = __builtin_amdgcn_permlane32_swap(__float_as_uint(a), __float_as_uint(a), false, false);
  return fmx(__uint_as_float(r[0]), __uint_as_float(r[1]));
}
__device__ __forceinline__ float wave_sum2(float a, float b) {
  float x = sw32_add(a, b);
  x = sw16_add(x, x);
  x += dpp<XOR1>(x); x += dpp<XOR2>(x); x += dpp<ROR8>(x); x += dpp<ROR4>(x);
  return x;
}
__device__ __forceinline__ rsrc_t mk(const void* p, u32 bytes) { return __builtin_amdgcn_make_buffer_rsrc((void*)p, 0, bytes, 0x00020000); }
#define LD128(r, v, s, a) __builtin_amdgcn_raw_buffer_load_b128(r, v, s, a)
#define LD64(r, v, s, a) __builtin_amdgcn_raw_buffer_load_b64(r, v, s, a)
#define LD32(r, v, s, a) __builtin_amdgcn_raw_buffer_load_b32(r, v, s, a)
#define ST128(d, r, v, s, a) __builtin_amdgcn_raw_buffer_store_b128(d, r, v, s, a)
#define ST64(d, r, v, s, a) __builtin_amdgcn_raw_buffer_store_b64(d, r, v, s, a)
#define ST32(d, r, v, s, a) __builtin_amdgcn_raw_buffer_store_b32(d, r, v, s, a)
__device__ __forceinline__ u32 canon(float f) { return min(__float_as_uint(f), 0xFFFFFFFEu); }
__device__ __forceinline__ u32x4 canon4(f32x4 v) { return u32x4{canon(v[0]), canon(v[1]), canon(v[2]), canon(v[3])}; }
__device__ __forceinline__ u32 max4(u32x4 v) { return max(max(v[0], v[1]), max(v[2], v[3])); }
__device__ __forceinline__ f32x4 asf(u32x4 v) { return __builtin_bit_cast(f32x4, v); }
__device__ __forceinline__ float rcp(float x) { return __builtin_amdgcn_rcpf(x); }
__device__ __forceinline__ float silu(float x) { return x * rcp(1.f + __expf(-x)); }
__device__ __forceinline__ float dot2(u32 a, u32 b, float c) { return __builtin_amdgcn_fdot2_f32_bf16(__builtin_bit_cast(bf16x2, a), __builtin_bit_cast(bf16x2, b), c, false); }
#define MEMF() asm volatile("" ::: "memory")
#define LGKM0() asm volatile("s_waitcnt lgkmcnt(0)" ::: "memory")
#define VM0() asm volatile("s_waitcnt vmcnt(0)" ::: "memory")
#define BAR() do { LGKM0(); __builtin_amdgcn_s_barrier(); MEMF(); } while (0)
#define SB() __builtin_amdgcn_sched_barrier(0)
#define PACE() asm volatile("s_waitcnt vmcnt(%0)" :: "i"(WIN - 1) : "memory")
#define PIN(x) asm volatile("" : "+v"(x))
__device__ __forceinline__ u32 lds_inc(LAS unsigned char* p) { return __hip_atomic_fetch_add((LAS u32*)p, 1u, __ATOMIC_RELAXED, __HIP_MEMORY_SCOPE_WORKGROUP); }
__device__ __forceinline__ void lds_wait_ge(LAS unsigned char* p, u32 n) {
#pragma nounroll
  for (int sp = 0; sp < SPIN_CAP * 16; ++sp) { if (*(volatile LAS u32*)p >= n) break; __builtin_amdgcn_s_sleep(1); }
  MEMF();
}
__device__ __forceinline__ void ln_fin(float c, float S1, float S2, float& m, float& den) {
  const float d = S1 * (1.f / D);
  const float var = rbf(fmaxf(fmaf(-d, d, S2 * (1.f / D)), 0.f));
  m = rbf(c + d); den = rcp(rbf(__builtin_amdgcn_sqrtf(rbf(var + 1e-5f))));
}
__device__ __forceinline__ void gemv32(const u32 (&wp)[32], const u32 (&hp)[4], float& r0, float& r1) {
  float a[8];
#pragma unroll
  for (int j = 0; j < 8; ++j) a[j] = dot2(wp[24 + j], hp[3], dot2(wp[16 + j], hp[2], dot2(wp[8 + j], hp[1], dot2(wp[j], hp[0], 0.f))));
#pragma unroll
  for (int j = 0; j < 4; ++j) a[j] = sw32_add(a[j], a[j + 4]);
#pragma unroll
  for (int j = 0; j < 2; ++j) a[j] = sw16_add(a[j], a[j + 2]);
#pragma unroll
  for (int j = 0; j < 2; ++j) { a[j] += dpp<ROR8>(a[j]); a[j] += dpp<ROR4>(a[j]); }
  r0 = a[0]; r1 = a[1];
}
#define PAIR_LO(hi, lo) __builtin_amdgcn_perm(hi, lo, 0x05040100u)
#define PAIR_HI(hi, lo) __builtin_amdgcn_perm(hi, lo, 0x07060302u)
#define PH_ISSUE(vo_, so_) asm volatile("s_nop 4\n\t" \
               "buffer_load_dwordx4 v[100:103], %[vo], %[rs], %[so] offen sc1\n\t" \
                : "+{v[100:103]}"(P[0]) : [vo] "v"(vo_), [so] "s"(so_), [rs] "s"(rws) : "memory")
#define PH_WAIT(n) asm volatile("s_waitcnt vmcnt(" #n ")" : "+{v[100:103]}"(P[0]) :: "memory")
#define PH_TIE() asm volatile("" : "+{v[100:103]}"(P[0]))
#define PQ_ISSUE(vo_) asm volatile("s_nop 4\n\t" \
               "buffer_load_dwordx4 v[100:103], %[vo], %[rs], 0 offen sc1\n\t" \
               "buffer_load_dwordx4 v[104:107], %[vo], %[rs], 0 offen offset:1024 sc1\n\t" \
                : "+{v[100:103]}"(P[0]), "+{v[104:107]}"(P[1]) : [vo] "v"(vo_), [rs] "s"(rws) : "memory")
#define PQ_WAIT(n) asm volatile("s_waitcnt vmcnt(" #n ")" : "+{v[100:103]}"(P[0]), "+{v[104:107]}"(P[1]) :: "memory")
#define PQ_TIE() asm volatile("" : "+{v[100:103]}"(P[0]), "+{v[104:107]}"(P[1]))
#define PM_ISSUE(vm_) asm volatile("s_nop 4\n\t" \
               "buffer_load_dwordx4 v[116:119], %[vm], %[rs], 0 offen sc1\n\t" \
                : "+{v[116:119]}"(P[4]) : [vm] "v"(vm_), [rs] "s"(rws) : "memory")
#define PM_WAIT(n) asm volatile("s_waitcnt vmcnt(" #n ")" : "+{v[116:119]}"(P[4]) :: "memory")
#define PM_TIE() asm volatile("" : "+{v[116:119]}"(P[4]))
#define P1_ISSUE(vo_) asm volatile("s_nop 4\n\t" \
               "buffer_load_dwordx4 v[100:103], %[vo], %[rs], 0 offen sc1\n\t" \
               "buffer_load_dwordx4 v[104:107], %[vo], %[rs], 0 offen offset:2048 sc1\n\t" \
               "buffer_load_dwordx4 v[108:111], %[vo], %[rs], %[s1] offen sc1\n\t" \
               "buffer_load_dwordx4 v[112:115], %[vo], %[rs], %[s1] offen offset:2048 sc1\n\t" \
                : "+{v[100:103]}"(P[0]), "+{v[104:107]}"(P[1]), "+{v[108:111]}"(P[2]), "+{v[112:115]}"(P[3]) : [vo] "v"(vo_), [s1] "s"(4096), [rs] "s"(rws) : "memory")
#define P1_WAIT(n) asm volatile("s_waitcnt vmcnt(" #n ")" : "+{v[100:103]}"(P[0]), "+{v[104:107]}"(P[1]), "+{v[108:111]}"(P[2]), "+{v[112:115]}"(P[3]) :: "memory")
#define P1_TIE() asm volatile("" : "+{v[100:103]}"(P[0]), "+{v[104:107]}"(P[1]), "+{v[108:111]}"(P[2]), "+{v[112:115]}"(P[3]))
#define PX_ISSUE(vo_) asm volatile("s_nop 4\n\t" \
               "buffer_load_dwordx4 v[100:103], %[vo], %[rs], 0 offen sc1\n\t" \
               "buffer_load_dwordx4 v[104:107], %[vo], %[rs], 0 offen offset:1024 sc1\n\t" \
                : "+{v[100:103]}"(P[0]), "+{v[104:107]}"(P[1]) : [vo] "v"(vo_), [rs] "s"(rws) : "memory")
#define PX_WAIT(n) asm volatile("s_waitcnt vmcnt(" #n ")" : "+{v[100:103]}"(P[0]), "+{v[104:107]}"(P[1]) :: "memory")
#define PX_TIE() asm volatile("" : "+{v[100:103]}"(P[0]), "+{v[104:107]}"(P[1]))
#define P2_SUMS(r0_, vo_) { int cnt_ = SPIN_CAP / 4, np_, tt_; f32x4 t0_, t1_; asm volatile( \
      "s_nop 4\n\t" \
      "4:\n\t" \
      "buffer_load_dwordx4 v[100:103], %[vo], %[rs], 0 offen sc1\n\t" \
      "buffer_load_dwordx4 v[104:107], %[vo], %[rs], 0 offen offset:1024 sc1\n\t" \
      "buffer_load_dwordx4 v[108:111], %[vo], %[rs], 0 offen offset:2048 sc1\n\t" \
      "buffer_load_dwordx4 v[112:115], %[vo], %[rs], 0 offen offset:3072 sc1\n\t" \
      "1:\n\t" \
      "s_waitcnt vmcnt(0)\n\t" \
      "v_pk_add_f32 v[92:93], v[100:101], v[104:105]\n\t" \
      "v_pk_add_f32 v[94:95], v[102:103], v[106:107]\n\t" \
      "v_pk_add_f32 v[84:85], v[108:109], v[112:113]\n\t" \
      "v_pk_add_f32 v[86:87], v[110:111], v[114:115]\n\t" \
      "v_pk_add_f32 v[92:93], v[92:93], v[84:85]\n\t" \
      "v_pk_add_f32 v[94:95], v[94:95], v[86:87]\n\t" \
      "s_nop 0\n\t" \
      "v_pk_add_f32 v[84:85], v[92:93], v[94:95]\n\t" \
      "s_nop 0\n\t" \
      "v_cmp_u_f32 vcc, v84, v85\n\t" \
      "s_cbranch_vccz 2f\n\t" \
      "s_sub_u32 %[cnt], %[cnt], 1\n\t" \
      "s_cbranch_scc1 2f\n\t" \
      "s_mov_b32 %[np], 0\n\t" \
      "v_cmp_eq_u32 vcc, -1, v100\n\t" \
      "s_bcnt1_i32_b64 %[t], vcc\n\t" \
      "s_add_u32 %[np], %[np], %[t]\n\t" \
      "v_cmp_eq_u32 vcc, -1, v104\n\t" \
      "s_bcnt1_i32_b64 %[t], vcc\n\t" \
      "s_add_u32 %[np], %[np], %[t]\n\t" \
      "v_cmp_eq_u32 vcc, -1, v108\n\t" \
      "s_bcnt1_i32_b64 %[t], vcc\n\t" \
      "s_add_u32 %[np], %[np], %[t]\n\t" \
      "v_cmp_eq_u32 vcc, -1, v112\n\t" \
      "s_bcnt1_i32_b64 %[t], vcc\n\t" \
      "s_add_u32 %[np], %[np], %[t]\n\t" \
      "s_cmp_eq_u32 %[np], 0\n\t" \
      "s_cbranch_scc1 4b\n\t" \
      "s_cmp_gt_u32 %[np], %[hi]\n\t" \
      "s_cbranch_scc0 3f\n\t" \
      "s_sleep %[slow]\n\t" \
      "3:\n\t" \
      "s_sleep %[gap]\n\t" \
      "v_cmp_eq_u32 vcc, -1, v100\n\t" \
      "s_mov_b64 exec, vcc\n\t" \
      "s_cbranch_execz 5f\n\t" \
      "buffer_load_dwordx4 v[100:103], %[vo], %[rs], 0 offen sc1\n\t" \
      "5:\n\t" \
      "s_mov_b64 exec, -1\n\t" \
      "v_cmp_eq_u32 vcc, -1, v104\n\t" \
      "s_mov_b64 exec, vcc\n\t" \
      "s_cbranch_execz 5f\n\t" \
      "buffer_load_dwordx4 v[104:107], %[vo], %[rs], 0 offen offset:1024 sc1\n\t" \
      "5:\n\t" \
      "s_mov_b64 exec, -1\n\t" \
      "v_cmp_eq_u32 vcc, -1, v108\n\t" \
      "s_mov_b64 exec, vcc\n\t" \
      "s_cbranch_execz 5f\n\t" \
      "buffer_load_dwordx4 v[108:111], %[vo], %[rs], 0 offen offset:2048 sc1\n\t" \
      "5:\n\t" \
      "s_mov_b64 exec, -1\n\t" \
      "v_cmp_eq_u32 vcc, -1, v112\n\t" \
      "s_mov_b64 exec, vcc\n\t" \
      "s_cbranch_execz 5f\n\t" \
      "buffer_load_dwordx4 v[112:115], %[vo], %[rs], 0 offen offset:3072 sc1\n\t" \
      "5:\n\t" \
      "s_mov_b64 exec, -1\n\t" \
      "s_branch 1b\n\t" \
      "2:\n\t" "s_nop 0" \
      : "+{v[100:103]}"(P[0]), "+{v[104:107]}"(P[1]), "+{v[108:111]}"(P[2]), "+{v[112:115]}"(P[3]), "=&{v[92:95]}"(r0_), "=&{v[84:87]}"(t0_), "=&{v[88:91]}"(t1_), [cnt] "+&s"(cnt_), [np] "=&s"(np_), [t] "=&s"(tt_) \
      : [vo] "v"(vo_), [rs] "s"(rws), [gap] "i"(POLL_GAP), [slow] "i"(POLL_SLOW), [hi] "i"(POLL_HI) : "vcc", "scc", "memory"); DBGCNT(21, SPIN_CAP / 4 - cnt_ + 1); }
#define P3_SUMS(r0_, vo_, v3_) { int cnt_ = SPIN_CAP / 4, np_, tt_; f32x4 t0_, t1_; asm volatile( \
      "s_nop 4\n\t" \
      "4:\n\t" \
      "buffer_load_dwordx4 v[100:103], %[vo], %[rs], 0 offen sc1\n\t" \
      "buffer_load_dwordx4 v[104:107], %[vo], %[rs], %[s1] offen sc1\n\t" \
      "buffer_load_dwordx4 v[108:111], %[vo], %[rs], %[s2] offen sc1\n\t" \
      "buffer_load_dwordx4 v[112:115], %[v3], %[rs], 0 offen sc1\n\t" \
      "1:\n\t" \
      "s_waitcnt vmcnt(0)\n\t" \
      "v_pk_add_f32 v[92:93], v[100:101], v[104:105]\n\t" \
      "v_pk_add_f32 v[94:95], v[102:103], v[106:107]\n\t" \
      "v_pk_add_f32 v[84:85], v[108:109], v[112:113]\n\t" \
      "v_pk_add_f32 v[86:87], v[110:111], v[114:115]\n\t" \
      "v_pk_add_f32 v[92:93], v[92:93], v[84:85]\n\t" \
      "v_pk_add_f32 v[94:95], v[94:95], v[86:87]\n\t" \
      "s_nop 0\n\t" \
      "v_pk_add_f32 v[84:85], v[92:93], v[94:95]\n\t" \
      "s_nop 0\n\t" \
      "v_cmp_u_f32 vcc, v84, v85\n\t" \
      "s_cbranch_vccz 2f\n\t" \
      "s_sub_u32 %[cnt], %[cnt], 1\n\t" \
      "s_cbranch_scc1 2f\n\t" \
      "s_mov_b32 %[np], 0\n\t" \
      "v_cmp_eq_u32 vcc, -1, v100\n\t" \
      "s_bcnt1_i32_b64 %[t], vcc\n\t" \
      "s_add_u32 %[np], %[np], %[t]\n\t" \
      "v_cmp_eq_u32 vcc, -1, v104\n\t" \
      "s_bcnt1_i32_b64 %[t], vcc\n\t" \
      "s_add_u32 %[np], %[np], %[t]\n\t" \
      "v_cmp_eq_u32 vcc, -1, v108\n\t" \
      "s_bcnt1_i32_b64 %[t], vcc\n\t" \
      "s_add_u32 %[np], %[np], %[t]\n\t" \
      "v_cmp_eq_u32 vcc, -1, v112\n\t" \
      "s_bcnt1_i32_b64 %[t], vcc\n\t" \
      "s_add_u32 %[np], %[np], %[t]\n\t" \
      "s_cmp_eq_u32 %[np], 0\n\t" \
      "s_cbranch_scc1 4b\n\t" \
      "s_cmp_gt_u32 %[np], %[hi]\n\t" \
      "s_cbranch_scc0 3f\n\t" \
      "s_sleep %[slow]\n\t" \
      "3:\n\t" \
      "s_sleep %[gap]\n\t" \
      "v_cmp_eq_u32 vcc, -1, v100\n\t" \
      "s_mov_b64 exec, vcc\n\t" \
      "s_cbranch_execz 5f\n\t" \
      "buffer_load_dwordx4 v[100:103], %[vo], %[rs], 0 offen sc1\n\t" \
      "5:\n\t" \
      "s_mov_b64 exec, -1\n\t" \
      "v_cmp_eq_u32 vcc, -1, v104\n\t" \
      "s_mov_b64 exec, vcc\n\t" \
      "s_cbranch_execz 5f\n\t" \
      "buffer_load_dwordx4 v[104:107], %[vo], %[rs], %[s1] offen sc1\n\t" \
      "5:\n\t" \
      "s_mov_b64 exec, -1\n\t" \
      "v_cmp_eq_u32 vcc, -1, v108\n\t" \
      "s_mov_b64 exec, vcc\n\t" \
      "s_cbranch_execz 5f\n\t" \
      "buffer_load_dwordx4 v[108:111], %[vo], %[rs], %[s2] offen sc1\n\t" \
      "5:\n\t" \
      "s_mov_b64 exec, -1\n\t" \
      "v_cmp_eq_u32 vcc, -1, v112\n\t" \
      "s_mov_b64 exec, vcc\n\t" \
      "s_cbranch_execz 5f\n\t" \
      "buffer_load_dwordx4 v[112:115], %[v3], %[rs], 0 offen sc1\n\t" \
      "5:\n\t" \
      "s_mov_b64 exec, -1\n\t" \
      "s_branch 1b\n\t" \
      "2:\n\t" "s_nop 0" \
      : "+{v[100:103]}"(P[0]), "+{v[104:107]}"(P[1]), "+{v[108:111]}"(P[2]), "+{v[112:115]}"(P[3]), "=&{v[92:95]}"(r0_), "=&{v[84:87]}"(t0_), "=&{v[88:91]}"(t1_), [cnt] "+&s"(cnt_), [np] "=&s"(np_), [t] "=&s"(tt_) \
      : [vo] "v"(vo_), [v3] "v"(v3_), [s1] "s"(8192), [s2] "s"(16384), [rs] "s"(rws), [gap] "i"(POLL_GAP), [slow] "i"(POLL_SLOW), [hi] "i"(POLL_HI) : "vcc", "scc", "memory"); DBGCNT(16, SPIN_CAP / 4 - cnt_ + 1); }
#define KEEP() asm volatile("; keep" :: "{v[100:103]}"(P[0]), "{v[104:107]}"(P[1]), "{v[108:111]}"(P[2]), "{v[112:115]}"(P[3]), "{v[116:119]}"(P[4]), "{v[120:123]}"(P[5]))
#define POOL_INIT() asm volatile("; pool" : "={v[100:103]}"(P[0]), "={v[104:107]}"(P[1]), "={v[108:111]}"(P[2]), "={v[112:115]}"(P[3]), "={v[116:119]}"(P[4]), "={v[120:123]}"(P[5]))

#define POLL(NAME, N, PEND, ...)                     \
  NAME##_ISSUE(__VA_ARGS__); NAME##_WAIT(0);                                                      \
  { bool pend_ = PEND;                                                                            \
    if (__any(pend_)) {                                                                           \
      if (pend_) NAME##_ISSUE(__VA_ARGS__);                                                       \
      __builtin_amdgcn_s_sleep(4);                                                                \
      _Pragma("nounroll") for (int sp = 0; sp < SPIN_CAP; ++sp) {                                 \
        if (pend_) NAME##_ISSUE(__VA_ARGS__);                                                     \
        NAME##_WAIT(N); pend_ = PEND;                                                             \
        if (!__any(pend_)) break;                                                                 \
      } }                                                                                         \
    NAME##_TIE(); }
#define POLLB(NAME, HI, PEND, ...)     \
  NAME##_ISSUE(__VA_ARGS__);                                                                      \
  _Pragma("nounroll") for (int sp = 0; sp < SPIN_CAP; ++sp) {                                     \
    NAME##_WAIT(0);                                                                               \
    DBGCNT(20, sp + 1);                                                                           \
    const bool pend_ = PEND;                                                                      \
    const u64 bal_ = __ballot(pend_);                                                             \
    if (bal_ == 0) break;                                                                         \
    if (__popcll(bal_) > HI) __builtin_amdgcn_s_sleep(POLL_SLOW);                                 \
    __builtin_amdgcn_s_sleep(POLL_GAP);                                                           \
    if (pend_) NAME##_ISSUE(__VA_ARGS__);                                                         \
  }

__global__ void __launch_bounds__(768, 3)
fused_block(const u16* __restrict__ gx, const u16* __restrict__ gk, const u16* __restrict__ gv, const u16* __restrict__ gwq,
            const u16* __restrict__ gwo, const u16* __restrict__ gwg, const u16* __restrict__ gwu, const u16* __restrict__ gwd,
            const u16* __restrict__ gc, const u16* __restrict__ gwm, u16* __restrict__ gout, unsigned char* __restrict__ gws) {
  extern __shared__ __attribute__((aligned(16))) unsigned char smem[];
  LAS unsigned char* lds = (LAS unsigned char*)smem;
  const int b = blockIdx.x, g = b & 7, s = b >> 3;
  const int t = threadIdx.x, w = __builtin_amdgcn_readfirstlane(t >> 6), l = t & 63, lg = l >> 4, lc = l & 15;
  const rsrc_t rws = mk(gws, WS_END);
  STAMPW(0, 0);
  asm volatile("" :: "s"(gx), "s"(gk), "s"(gv), "s"(gwq), "s"(gwo), "s"(gwg), "s"(gwu), "s"(gwd), "s"(gc), "s"(gwm), "s"(gout), "s"(gws));
  if (__builtin_expect(w >= 4, 1)) {
    const int a = w - 4;
    const size_t kvo = ((size_t)g * SK + s * 512) * DH;
    const rsrc_t rk = mk(gk + kvo, (ABL & 256) ? 0 : 131072), rv = mk(gv + kvo, (ABL & 256) ? 0 : 131072);
    {
      const int v0 = lg * 256 + ((lc ^ (4 * lg + 0)) * 16), v1 = lg * 256 + ((lc ^ (4 * lg + 1)) * 16);
      const int v2 = lg * 256 + ((lc ^ (4 * lg + 2)) * 16), v3 = lg * 256 + ((lc ^ (4 * lg + 3)) * 16);
#pragma unroll
      for (int T = 0; T < 4; ++T) {
        const int so = a * 16384 + T * 4096;
        if (T == 0)
          asm volatile("s_mov_b32 m0, %5\n\ts_nop 4\n\t"
                       "buffer_load_dwordx4 %0, %4, %5 offen nt lds\n\t"
                       "buffer_load_dwordx4 %1, %4, %5 offen offset:1024 nt lds\n\t"
                       "s_barrier\n\t"
                       "s_waitcnt vmcnt(%6)\n\tbuffer_load_dwordx4 %2, %4, %5 offen offset:2048 nt lds\n\t"
                       "s_waitcnt vmcnt(%6)\n\tbuffer_load_dwordx4 %3, %4, %5 offen offset:3072 nt lds"
                       :: "v"(v0), "v"(v1), "v"(v2), "v"(v3), "s"(rk), "s"(so), "i"(WIN - 1) : "memory");
        else
          asm volatile("s_mov_b32 m0, %5\n\ts_nop 4\n\t"
                       "s_waitcnt vmcnt(%6)\n\tbuffer_load_dwordx4 %0, %4, %5 offen nt lds\n\t"
                       "s_waitcnt vmcnt(%6)\n\tbuffer_load_dwordx4 %1, %4, %5 offen offset:1024 nt lds\n\t"
                       "s_waitcnt vmcnt(%6)\n\tbuffer_load_dwordx4 %2, %4, %5 offen offset:2048 nt lds\n\t"
                       "s_waitcnt vmcnt(%6)\n\tbuffer_load_dwordx4 %3, %4, %5 offen offset:3072 nt lds"
                       :: "v"(v0), "v"(v1), "v"(v2), "v"(v3), "s"(rk), "s"(so), "i"(WIN - 1) : "memory");
      }
    }
    u32x4 vf[16];
#define VLOAD(i) vf[i] = LD128(rv, l * 16 + ((i) & 3) * 1024, a * 16384 + ((i) >> 2) * 4096, AUX_NT)
#pragma unroll
    for (int i = 0; i < QPOS; ++i) { PACE(); VLOAD(i); if (i == WARM_AT && l == 0) lds_inc(lds + L_FLAG + F_WARM); }
    SB();
    const u32 hm = (lc & 1) ? 0xffff0000u : 0x0000ffffu;
    u32 m0 = (lc & 2) ? 0u : hm, m1 = (lc & 2) ? hm : 0u;
    int ka[4], pubo = WS_PO + (((4 * g + (a & 3)) * 8 + (l >> 3)) * 32 + s) * 128 + (l & 7) * 16;
#pragma unroll
    for (int tt = 0; tt < 4; ++tt) { ka[tt] = L_K + a * 16384 + (4 * (lc & 3) + (lc >> 2)) * 256 + (((4 * tt + lg) ^ lc) * 16); PIN(ka[tt]); }
    PIN(m0); PIN(m1); PIN(pubo);
    STAMPW(5, 4);
    lds_wait_ge(lds + L_FLAG + F_QRDY, 1u);
#pragma unroll
    for (int i = QPOS; i < 16; ++i) VLOAD(i);
    SB();
    if (l == 0) lds_inc(lds + L_FLAG + F_ISS);
    u32x4 qf[4];
#pragma unroll
    for (int tt = 0; tt < 4; ++tt) qf[tt] = *(LAS u32x4*)(lds + L_Q + (lc >> 2) * 256 + tt * 64 + lg * 16);
    asm volatile("s_waitcnt vmcnt(16)" ::: "memory");
    STAMPW(6, 4);
    f32x4 sc;
    {
      f32x4 c[4];
#pragma unroll
      for (int T = 0; T < 4; ++T) {
        c[T] = f32x4{0.f, 0.f, 0.f, 0.f};
#pragma unroll
        for (int tt = 0; tt < 4; ++tt)
          c[T] = __builtin_amdgcn_mfma_f32_16x16x32_bf16(__builtin_bit_cast(bf16x8, *(LAS u32x4*)(lds + ka[tt] + T * 4096)), __builtin_bit_cast(bf16x8, qf[tt]), c[T], 0, 0, 0);
      }
      const f32x4 lo = (l & 1) ? c[1] : c[0], hi = (l & 1) ? c[3] : c[2];
      sc = (l & 2) ? hi : lo;
    }
#pragma unroll
    for (int r = 0; r < 4; ++r) sc[r] = rbf(rbf(sc[r]) * 0.08838834764831845f);
    float mw = fmx(fmx(sc[0], sc[1]), fmx(sc[2], sc[3]));
    mw = fmx(mw, dpp<XOR1>(mw)); mw = fmx(mw, dpp<XOR2>(mw)); mw = sw32_max(sw16_max(mw));
    u32 pp[4];
    float lw = 0.f;
#pragma unroll
    for (int r = 0; r < 4; ++r) { const float p = __builtin_amdgcn_exp2f((sc[r] - mw) * L2E); lw += p; pp[r] = pk2(p, p); }
    f32x4 o0 = {0.f, 0.f, 0.f, 0.f}, o1 = {0.f, 0.f, 0.f, 0.f};
#define PV(i, Q) { const u32 x_ = dppu<Q>(pp[(i) & 3]); const u32x2 pa_ = {x_ & m0, x_ & m1};                                                         \
      o0 = __builtin_amdgcn_mfma_f32_16x16x16bf16_1k(__builtin_bit_cast(s16x4, pa_), __builtin_bit_cast(s16x4, u32x2{vf[i][0], vf[i][1]}), o0, 0, 0, 0); \
      o1 = __builtin_amdgcn_mfma_f32_16x16x16bf16_1k(__builtin_bit_cast(s16x4, pa_), __builtin_bit_cast(s16x4, u32x2{vf[i][2], vf[i][3]}), o1, 0, 0, 0); }
    PV(0, 0x00) PV(1, 0x00) PV(2, 0x00) PV(3, 0x00)
    lw += dpp<XOR1>(lw); lw += dpp<XOR2>(lw); lw = sw16_add(lw, lw); lw = sw32_add(lw, lw);
    if (lg == 0 && (lc & 3) == 0) { *(LAS float*)(lds + L_M + (lc >> 2) * 32 + a * 4) = mw; *(LAS float*)(lds + L_L + (lc >> 2) * 32 + a * 4) = lw; }
    LGKM0();
    if (l == 0) lds_inc(lds + L_FLAG + F_SM);
    PV(4, 0x55) PV(5, 0x55) PV(6, 0x55) PV(7, 0x55) PV(8, 0xAA) PV(9, 0xAA) PV(10, 0xAA) PV(11, 0xAA)
    float e[8], M = 0.f, L = 0.f;
    if (a < 4) {
      lds_wait_ge(lds + L_FLAG + F_SM, 8u);
      const f32x4 ma = *(LAS f32x4*)(lds + L_M + a * 32), mb = *(LAS f32x4*)(lds + L_M + a * 32 + 16);
      const f32x4 la = *(LAS f32x4*)(lds + L_L + a * 32), lb = *(LAS f32x4*)(lds + L_L + a * 32 + 16);
      M = fmx(fmx(fmx(ma[0], ma[1]), fmx(ma[2], ma[3])), fmx(fmx(mb[0], mb[1]), fmx(mb[2], mb[3])));
#pragma unroll
      for (int i = 0; i < 8; ++i) {
        e[i] = __builtin_amdgcn_exp2f(((i < 4 ? ma[i & 3] : mb[i & 3]) - M) * L2E);
        L = fmaf(e[i], i < 4 ? la[i & 3] : lb[i & 3], L);
        PIN(e[i]);
      }
      PIN(L);
    }
    SB();
    PV(12, 0xFF) PV(13, 0xFF) PV(14, 0xFF) PV(15, 0xFF)
    __builtin_amdgcn_s_setprio(3);
    *(LAS f32x4*)(lds + L_ACC + a * 2048 + lg * 512 + lc * 32) = o0;
    *(LAS f32x4*)(lds + L_ACC + a * 2048 + lg * 512 + lc * 32 + 16) = o1;
    LGKM0();
    if (l == 0) lds_inc(lds + L_FLAG + F_ATT);
    STAMPW(7, 11); STAMPW(23, 4);
    if (a < 4) {
      const int ao = L_ACC + a * 512 + ((l >> 3) * 4 + (l & 3)) * 16;
      lds_wait_ge(lds + L_FLAG + F_ATT, 8u);
      f32x4 oa = e[0] * *(LAS f32x4*)(lds + ao), ob = e[1] * *(LAS f32x4*)(lds + ao + 2048);
#pragma unroll
      for (int i = 2; i < 8; i += 2) { oa += e[i] * *(LAS f32x4*)(lds + ao + i * 2048); ob += e[i + 1] * *(LAS f32x4*)(lds + ao + (i + 1) * 2048); }
      f32x4 o = oa + ob;
      if (l & 4) o = f32x4{M, L, 0.f, 0.f};
      ST128(canon4(o), rws, pubo, 0, AUX_SC1);
      STAMPW(8, 4);
    }
  } else {
    const rsrc_t rx = mk(gx, 1024);
    const int hh = 4 * g + (s >> 3), cs = s & 7, uj = 28 * g + s, fin = 4 * g + s;
    const bool ffn = s < 28, red = s == 28 || s == 29;
    const int rid = 2 * g + (s & 1);
    u32 tag, arr;
    u32x4 P[6];
    POOL_INIT();
#define TAGS(v) ((v[1] ^ tag) | (v[3] ^ tag))
    {
      const bool qjob = s >= 16, mjob = s >= 4 && s < 16;
      const int job = qjob ? 16 * g + (s - 16) : 12 * g + (s - 4);
      const int pitch = qjob ? 8192 : 6144;
      const rsrc_t rc = mk(gc, 1024), rw = mk(qjob ? gwq : gwm, qjob ? ((ABL & 8) ? 0 : 512 * 8192) : (mjob && !(ABL & 4) ? 512 * 6144 : 0));
      u32x4 wv[8];
#pragma unroll
      for (int i = 0; i < 8; ++i) wv[i] = LD128(rw, (l >> 2) * 2 * pitch + (l & 3) * 16 + job * 64, (128 * w + (i >> 1) * 32 + (i & 1)) * pitch, AUX_NT);
      SB();
      u32 hp[4];
#pragma unroll
      for (int i = 0; i < 4; ++i) hp[i] = LD32(rc, (l >> 2) * 4 + i * 64, w * 256, 0);
      const u32 ep = LD32(rws, WS_EPOCH, 0, AUX_SC1);
      const u32x4 xv = LD128(rx, l * 16, 0, 0);
      const u32x2 xo = LD64(rx, (l & 31) * 8, w * 256, 0);
      SB();
      if (t < 8) *(volatile LAS u32*)(lds + L_FLAG + t * 4) = 0u;
      BAR();
      {
        const u32x4 sv = {SENT, SENT, SENT, SENT};
        const int pwo_ = WS_PW + ((2 * cs + (l >> 3)) * 32 + hh) * 128 + (l & 7) * 16;
        ST128(sv, rws, WS_PO + (((4 * g + w) * 8 + (l >> 3)) * 32 + s) * 128 + (l & 7) * 16, 0, AUX_SC1);
        const int o2 = w < 2 ? (ffn ? WS_PD + ((8 * w + (l >> 3)) * 224 + uj) * 128 + (l & 7) * 16 : OOB) : (w == 2 ? (l < 16 ? pwo_ : OOB) : (red && l < 8 ? WS_X1 + rid * 128 + l * 16 : OOB));
        ST128(sv, rws, o2, 0, AUX_SC1);
      }
      SB();
      tag = __builtin_amdgcn_readfirstlane(ep) + 1u;
      float xn[4];
      if (qjob) {
        float xs[8];
#pragma unroll
        for (int i = 0; i < 4; ++i) { xs[2 * i] = bfl(xv[i]); xs[2 * i + 1] = bfh(xv[i]); }
        const float c = __uint_as_float(__builtin_amdgcn_readfirstlane(__float_as_uint(xs[0])));
        float s1 = 0.f, s2 = 0.f;
#pragma unroll
        for (int i = 0; i < 8; ++i) { const float d = xs[i] - c; s1 += d; s2 = fmaf(d, d, s2); }
        const float ss = wave_sum2(s1, s2);
        float m, den;
        ln_fin(c, __uint_as_float(__builtin_amdgcn_readlane(__float_as_uint(ss), 0)), __uint_as_float(__builtin_amdgcn_readlane(__float_as_uint(ss), 32)), m, den);
        xn[0] = rbf(rbf(bfl(xo[0]) - m) * den); xn[1] = rbf(rbf(bfh(xo[0]) - m) * den);
        xn[2] = rbf(rbf(bfl(xo[1]) - m) * den); xn[3] = rbf(rbf(bfh(xo[1]) - m) * den);
#pragma unroll
        for (int i = 0; i < 4; ++i) PIN(xn[i]);
      } else {
#pragma unroll
        for (int i = 0; i < 4; ++i) { hp[i] = pk2(silu(bfl(hp[i])), silu(bfh(hp[i]))); PIN(hp[i]); }
      }
      u32 wp[32];
#pragma unroll
      for (int ip = 0; ip < 4; ++ip)
#pragma unroll
        for (int j = 0; j < 4; ++j) { wp[8 * ip + 2 * j] = PAIR_LO(wv[2 * ip + 1][j], wv[2 * ip][j]); wp[8 * ip + 2 * j + 1] = PAIR_HI(wv[2 * ip + 1][j], wv[2 * ip][j]); }
      if (qjob) {
#pragma unroll
        for (int i = 0; i < 32; ++i) PIN(wp[i]);
        const int vo = WS_MG + (l >> 5) * 2048 + (l & 31) * 16, so = w * 512;
        POLL(PH, 1, TAGS(P[0]) != 0u && !(ABL & 16), vo, so)
        STAMPW(2, 0);
        const auto e0 = __builtin_amdgcn_permlane32_swap(P[0][0], P[0][0], false, false), e1 = __builtin_amdgcn_permlane32_swap(P[0][2], P[0][2], false, false);
        const u32 h01 = pk2(rbf(xn[0] * rbf(1.f + bfl(e0[1]))) + bfl(e0[0]), rbf(xn[1] * rbf(1.f + bfh(e0[1]))) + bfh(e0[0]));
        const u32 h23 = pk2(rbf(xn[2] * rbf(1.f + bfl(e1[1]))) + bfl(e1[0]), rbf(xn[3] * rbf(1.f + bfh(e1[1]))) + bfh(e1[0]));
        *(LAS u32x2*)(lds + L_H + w * 256 + (l & 31) * 8) = u32x2{h01, h23};
#pragma unroll
        for (int i = 0; i < 4; ++i) hp[i] = *(LAS u32*)(lds + L_H + w * 256 + i * 64 + (l >> 2) * 4);
      }
      float r0, r1;
      gemv32(wp, hp, r0, r1);
      if (lc < 4) *(LAS f32x2*)(lds + L_FM + w * 128 + lc * 32 + lg * 8) = f32x2{r0, r1};
      if (!qjob) VM0();
      LGKM0();
      u32 old = 0;
      if (l == 0) old = lds_inc(lds + L_FLAG + F_CQ);
      arr = __builtin_amdgcn_readfirstlane(old);
      if (arr == 3u) {
        const int ro = L_FM + (l & 7) * 16;
        const f32x4 v = (*(LAS f32x4*)(lds + ro) + *(LAS f32x4*)(lds + ro + 128)) + (*(LAS f32x4*)(lds + ro + 256) + *(LAS f32x4*)(lds + ro + 384));
        ST128((u32x4{pk2(v[0], v[1]), tag, pk2(v[2], v[3]), tag}), rws, l < 8 && s >= 4 ? (qjob ? WS_QG : WS_MG) + job * 128 + l * 16 : OOB, 0, AUX_SC1);
        ST32(tag, rws, l == 0 ? WS_RDY + b * 4 : OOB, 0, AUX_SC1);
        if (qjob) STAMP(3); else if (mjob && job < 32) STAMP(1);
        if (!qjob) {
#pragma nounroll
          for (int i = 0; i < QG_DELAY; ++i) __builtin_amdgcn_s_sleep(16);
        }
        const int vo = WS_QG + g * 2048 + l * 16;
        POLL(PQ, 2, (TAGS(P[0]) | TAGS(P[1])) != 0u && !(ABL & 32), vo)
        *(LAS u32x2*)(lds + L_Q + l * 8) = u32x2{P[0][0], P[0][2]};
        *(LAS u32x2*)(lds + L_Q + 512 + l * 8) = u32x2{P[1][0], P[1][2]};
        LGKM0();
        *(volatile LAS u32*)(lds + L_FLAG + F_QRDY) = 1u;
        STAMP(4);
#pragma nounroll
        for (int sp = 0; sp < SPIN_CAP; ++sp) {
          MEMF();
          const u32x4 rd = LD128(rws, WS_RDY + l * 16, 0, AUX_SC1);
          if (((rd[0] ^ tag) | (rd[1] ^ tag) | (rd[2] ^ tag) | (rd[3] ^ tag)) == 0u) break;
          __builtin_amdgcn_s_sleep(4);
        }
        *(volatile LAS u32*)(lds + L_FLAG + F_RDY) = 1u;
        STAMP(19);
      }
    }
    u32 warm = 0;
    if (arr < 2u && !(ABL & 128)) {
      const rsrc_t rwx = mk(arr == 0u ? gwg : gwu, 512 * FF * 2);
      lds_wait_ge(lds + L_FLAG + F_WARM, 4u);
      warm = LD32(rwx, (16 * s + (l >> 2)) * (FF * 2) + (l & 3) * 128, (448 * g) & ~127, AUX_WARM);
    }
    SB();
    const int kA = 4 * l + w;
    float sbA, sbB, cbA, cbB, gbv;
    f32x4 ga4, x4;
    {
      const int mo = WS_MG + 6144 + (kA >> 1) * 8;
      u32x2 m2, m3, m4, m5, m6;
      u32x4 m0;
      const u32x2 xr = LD64(rx, rid * 64 + (l & 7) * 8, 0, 0);
      lds_wait_ge(lds + L_FLAG + F_QRDY, 1u);
#pragma nounroll
      for (int sp = 0; sp < SPIN_CAP; ++sp) {
        MEMF();
        m0 = LD128(rws, WS_MG + 4096 + rid * 128 + (l & 7) * 16, 0, AUX_SC1);
        m2 = LD64(rws, mo, 0, AUX_SC1); m3 = LD64(rws, mo + 1024, 0, AUX_SC1); m4 = LD64(rws, mo + 2048, 0, AUX_SC1); m5 = LD64(rws, mo + 3072, 0, AUX_SC1);
        m6 = LD64(rws, WS_MG + 10240 + ((fin & 31) >> 1) * 128 + (lc >> 2) * 32 + (fin & 1) * 16 + (lc & 2) * 4, 0, AUX_SC1);
        if ((TAGS(m0) | (m2[1] ^ tag) | (m3[1] ^ tag) | (m4[1] ^ tag) | (m5[1] ^ tag) | (m6[1] ^ tag)) == 0u) break;
        __builtin_amdgcn_s_sleep(4);
      }
      const bool hi = w & 1;
      sbA = hi ? bfh(m2[0]) : bfl(m2[0]); sbB = hi ? bfh(m3[0]) : bfl(m3[0]);
      cbA = rbf(1.f + (hi ? bfh(m4[0]) : bfl(m4[0]))); cbB = rbf(1.f + (hi ? bfh(m5[0]) : bfl(m5[0])));
      gbv = lc & 1 ? bfh(m6[0]) : bfl(m6[0]);
      ga4 = f32x4{bfl(m0[0]), bfh(m0[0]), bfl(m0[2]), bfh(m0[2])}; x4 = f32x4{bfl(xr[0]), bfh(xr[0]), bfl(xr[1]), bfh(xr[1])};
      PIN(sbA); PIN(sbB); PIN(cbA); PIN(cbB); PIN(gbv); PIN(ga4); PIN(x4);
    }
    const u32 hm = (lc & 1) ? 0xffff0000u : 0x0000ffffu;
    const int dw = (lc >> 1) & 3;
    u32 k0 = dw == 0 ? hm : 0u, k1 = dw == 1 ? hm : 0u, k2 = dw == 2 ? hm : 0u, k3 = dw == 3 ? hm : 0u;
    int p1m = l < 32 ? WS_PO + hh * 32768 + l * 128 + 64 : OOB, p1o = WS_PO + (hh * 8 + 2 * w) * 4096 + (l >> 2) * 128 + (l & 3) * 16;
    int r1w = (lc >> 3) == (lg >> 1) ? L_R1 + (2 * w + (lg >> 1)) * 256 + (lc & 7) * 32 + (lg & 1) * 16 : L_K + t * 16;
    int pwo = l < 16 ? WS_PW + ((2 * cs + (l >> 3)) * 32 + hh) * 128 + (l & 7) * 16 : OOB, p2o = WS_PW + rid * 4096 + l * 16;
    int x1o = l < 8 ? WS_X1 + rid * 128 + l * 16 : OOB, pxo = WS_X1 + l * 16;
    PIN(x1o); PIN(pxo);
    int r2w = (l & 6) == 0 ? L_R2 + ((l >> 3) * 2 + (l & 1)) * 16 + w * 4 : L_K + t * 16;
    int pdo = lg < 2 ? WS_PD + ((4 * w + (lc >> 2)) * 224 + uj) * 128 + lg * 64 + (lc & 3) * 16 : OOB;
    int p3o = WS_PD + ((fin & 31) >> 1) * (224 * 128) + (fin & 1) * 64 + (t >> 2) * 128 + (t & 3) * 16, p3x = t < 128 ? p3o + 3 * 8192 : OOB;
    int wdo = lg * 1024 + lc * 16;
    PIN(wdo); PIN(k0); PIN(k1); PIN(k2); PIN(k3); PIN(p1m); PIN(p1o); PIN(r1w); PIN(pwo); PIN(p2o); PIN(r2w); PIN(pdo); PIN(p3o); PIN(p3x);
#define DELTA(a) __builtin_bit_cast(bf16x8, u32x4{(a) & k0, (a) & k1, (a) & k2, (a) & k3})
    lds_wait_ge(lds + L_FLAG + F_ISS, 8u);
    u32x4 wov[4], wdv[2], wgA, wgB, wuA, wuB;
    {
      const rsrc_t rwo = mk(gwo, 4096 * 512 * 2);
#pragma unroll
      for (int n = 0; n < 4; ++n) wov[n] = LD128(rwo, (4 * (lc >> 3) + lg) * 1024 + (lc & 7) * 16, (hh * 128 + 32 * w + 8 * n) * 1024 + cs * 128, AUX_NT);
      SB();
      const u32 fb = ffn && !(ABL & 2) ? 512 * FF * 2 : 0;
      const rsrc_t rwg = mk(gwg, fb), rwu = mk(gwu, fb);
      wgA = LD128(rwg, kA * (FF * 2), uj * 16, AUX_NT); wgB = LD128(rwg, kA * (FF * 2), uj * 16 + 256 * FF * 2, AUX_NT);
      wuA = LD128(rwu, kA * (FF * 2), uj * 16, AUX_NT); wuB = LD128(rwu, kA * (FF * 2), uj * 16 + 256 * FF * 2, AUX_NT);
    }
    asm volatile("" :: "v"(warm));
    SB();
    STAMPW(22, 0);
    u32 gp[16];
#pragma unroll
    for (int j = 0; j < 4; ++j) {
      gp[2 * j] = PAIR_LO(wgB[j], wgA[j]); gp[2 * j + 1] = PAIR_HI(wgB[j], wgA[j]); gp[8 + 2 * j] = PAIR_LO(wuB[j], wuA[j]); gp[8 + 2 * j + 1] = PAIR_HI(wuB[j], wuA[j]);
    }
#pragma unroll
    for (int j = 0; j < 16; ++j) PIN(gp[j]);
    lds_wait_ge(lds + L_FLAG + F_RDY, 1u);
    lds_wait_ge(lds + L_FLAG + F_ATT, 8u);
    STAMPW(15, 0);
    {
      if (w == 0) { POLL(PM, 1, max(P[4][0], P[4][1]) == SENT, p1m) }
      STAMPW(17, 0);
      __builtin_amdgcn_s_barrier();
      P1_ISSUE(p1o);
      if (w == 0) {
        const float ms = l < 32 ? __uint_as_float(P[4][0]) : -INFINITY;
        float M = fmx(ms, dpp<XOR1>(ms));
        M = fmx(M, dpp<XOR2>(M)); M = fmx(M, dpp<ROR8>(M)); M = fmx(M, dpp<ROR4>(M)); M = sw16_max(M);
        const float e = __builtin_amdgcn_exp2f((ms - M) * L2E);
        float L = e * __uint_as_float(P[4][1]);
        L += dpp<XOR1>(L); L += dpp<XOR2>(L); L += dpp<ROR8>(L); L += dpp<ROR4>(L); L = sw16_add(L, L);
        if (l < 32) *(LAS float*)(lds + L_ST + l * 4) = e * rcp(L);
      }
      BAR();
      const float e0 = *(LAS float*)(lds + L_ST + (l >> 2) * 4), e1 = *(LAS float*)(lds + L_ST + 64 + (l >> 2) * 4);
#pragma nounroll
      for (int sp = 0; sp < SPIN_CAP; ++sp) {
        P1_WAIT(0);
        const bool pend = max(max(max4(P[0]), max4(P[1])), max(max4(P[2]), max4(P[3]))) == SENT;
        if (!__any(pend)) break;
        if (pend) P1_ISSUE(p1o);
      }
      STAMPW(9, 0);
      const f32x4 va = e0 * asf(P[0]) + e1 * asf(P[1]), vb = e0 * asf(P[2]) + e1 * asf(P[3]);
      float ya = sw16_add(sw32_add(va[0], va[2]), sw32_add(va[1], va[3])), yb = sw16_add(sw32_add(vb[0], vb[2]), sw32_add(vb[1], vb[3]));
      ya += dpp<ROR8>(ya); yb += dpp<ROR8>(yb); ya += dpp<ROR4>(ya); yb += dpp<ROR4>(yb);
      const u32 y2 = pk2(ya, yb);
      const u32 n0 = lc & 8 ? dppu<0x55>(y2) : dppu<0x00>(y2), n1 = lc & 8 ? dppu<0xFF>(y2) : dppu<0xAA>(y2);
      f32x4 d0 = {0.f, 0.f, 0.f, 0.f}, d1 = d0;
      d0 = __builtin_amdgcn_mfma_f32_16x16x32_bf16(DELTA(PAIR_LO(n0, n0)), __builtin_bit_cast(bf16x8, wov[0]), d0, 0, 0, 0);
      d1 = __builtin_amdgcn_mfma_f32_16x16x32_bf16(DELTA(PAIR_LO(n1, n1)), __builtin_bit_cast(bf16x8, wov[1]), d1, 0, 0, 0);
      d0 = __builtin_amdgcn_mfma_f32_16x16x32_bf16(DELTA(PAIR_HI(n0, n0)), __builtin_bit_cast(bf16x8, wov[2]), d0, 0, 0, 0);
      d1 = __builtin_amdgcn_mfma_f32_16x16x32_bf16(DELTA(PAIR_HI(n1, n1)), __builtin_bit_cast(bf16x8, wov[3]), d1, 0, 0, 0);
      *(LAS f32x4*)(lds + r1w) = d0 + d1;
      BAR();
      if (w == 0) {
        const f32x4 r = *(LAS f32x4*)(lds + L_R1 + (2 * lg) * 256 + lc * 16) + *(LAS f32x4*)(lds + L_R1 + (2 * lg + 1) * 256 + lc * 16);
        f32x4 q;
#pragma unroll
        for (int i = 0; i < 4; ++i) q[i] = sw16_add(r[i], r[i]);
#pragma unroll
        for (int i = 0; i < 4; ++i) q[i] = sw32_add(q[i], q[i]);
        ST128(canon4(q), rws, pwo, 0, AUX_SC1);
      }
      STAMPW(10, 0);
    }
    if (red && w == 1) {
      f32x4 v;
      P2_SUMS(v, p2o)
#pragma unroll
      for (int i = 0; i < 4; ++i) v[i] = sw32_add(v[i], v[i]);
#pragma unroll
      for (int i = 0; i < 4; ++i) v[i] = sw16_add(v[i], v[i]);
#pragma unroll
      for (int i = 0; i < 4; ++i) { v[i] += dpp<ROR8>(v[i]); v[i] = rbf(x4[i] + rbf(ga4[i] * rbf(v[i]))); }
      ST128(canon4(v), rws, x1o, 0, AUX_SC1);
      STAMP(18);
    }
    if (ffn) {
      {
        const rsrc_t rwd = mk(gwd, 512 * FF * 2);
        asm volatile("s_nop 4\n\tbuffer_load_dwordx4 %0, %2, %3, %4 offen nt\n\tbuffer_load_dwordx4 %1, %2, %3, %5 offen nt"
                     : "=&v"(wdv[0]), "=&v"(wdv[1]) : "v"(wdo), "s"(rwd), "s"(uj * 8192 + w * 256), "s"(uj * 8192 + 4096 + w * 256) : "memory");
      }
      if (w == 0) {
        POLL(PX, 2, max(max4(P[0]), max4(P[1])) == SENT, pxo)
        *(LAS u32x4*)(lds + L_XS + l * 16) = P[0]; *(LAS u32x4*)(lds + L_XS + 1024 + l * 16) = P[1];
      } else VM0();
      asm volatile("" : "+v"(wdv[0]), "+v"(wdv[1]));
      STAMPW(11, 0);
      BAR();
      const f32x4 xa = *(LAS f32x4*)(lds + L_XS + l * 16), xb = *(LAS f32x4*)(lds + L_XS + 1024 + l * 16);
      const float x1A = *(LAS float*)(lds + L_XS + kA * 4), x1B = *(LAS float*)(lds + L_XS + 1024 + kA * 4);
      const float cw = *(LAS float*)(lds + L_XS);
      const f32x4 da = xa - cw, db = xb - cw, d1 = da + db, d2 = da * da + db * db;
      const float ss = wave_sum2((d1[0] + d1[1]) + (d1[2] + d1[3]), (d2[0] + d2[1]) + (d2[2] + d2[3]));
      float m, den;
      ln_fin(cw, __uint_as_float(__builtin_amdgcn_readlane(__float_as_uint(ss), 0)), __uint_as_float(__builtin_amdgcn_readlane(__float_as_uint(ss), 32)), m, den);
      const u32 hp = pk2(rbf(rbf(rbf(x1A - m) * den) * cbA) + sbA, rbf(rbf(rbf(x1B - m) * den) * cbB) + sbB);
      float a[16];
#pragma unroll
      for (int j = 0; j < 16; ++j) a[j] = dot2(gp[j], hp, 0.f);
#pragma unroll
      for (int j = 0; j < 8; ++j) a[j] = sw32_add(a[j], a[j + 8]);
#pragma unroll
      for (int j = 0; j < 4; ++j) a[j] = sw16_add(a[j], a[j + 4]);
#pragma unroll
      for (int j = 0; j < 2; ++j) { const float lo = a[j] + dpp<ROR8>(a[j]), hi = a[j + 2] + dpp<ROR8>(a[j + 2]); a[j] = l & 8 ? hi : lo; }
      { const float lo = a[0] + dpp<XOR1>(a[0]), hi = a[1] + dpp<XOR1>(a[1]); a[0] = l & 1 ? hi : lo; }
      a[0] += dpp<XOR2>(a[0]);
      a[0] += dpp<0x141>(dpp<0x1B>(a[0]));
      *(LAS float*)(lds + r2w) = a[0];
      BAR();
      {
        float act[2];
#pragma unroll
        for (int n = 0; n < 2; ++n) {
          const int un = 4 * n + lg;
          const f32x4 gs = *(LAS f32x4*)(lds + L_R2 + un * 16), us = *(LAS f32x4*)(lds + L_R2 + (8 + un) * 16);
          const float gg = rbf((gs[0] + gs[1]) + (gs[2] + gs[3])), uu = rbf((us[0] + us[1]) + (us[2] + us[3]));
          const float z = gg * fmaf(gg * gg, -2.f * L2E * 0.7978845608028654f * 0.044715f, -2.f * L2E * 0.7978845608028654f);
          act[n] = rbf(gg * rcp(1.f + __builtin_amdgcn_exp2f(z))) * uu;
        }
        const u32 av = pk2(act[0], act[1]);
        f32x4 d = {0.f, 0.f, 0.f, 0.f};
        d = __builtin_amdgcn_mfma_f32_16x16x32_bf16(DELTA(PAIR_LO(av, av)), __builtin_bit_cast(bf16x8, wdv[0]), d, 0, 0, 0);
        d = __builtin_amdgcn_mfma_f32_16x16x32_bf16(DELTA(PAIR_HI(av, av)), __builtin_bit_cast(bf16x8, wdv[1]), d, 0, 0, 0);
        ST128(canon4(d), rws, pdo, 0, AUX_SC1);
      }
      STAMPW(12, 0);
      if (s < 4) {
        const int oc = 32 * (fin >> 1) + 8 * (lc >> 2) + 4 * (fin & 1) + (lc & 3);
        const float xo = *(LAS float*)(lds + L_XS + oc * 4);
        f32x4 v;
        P3_SUMS(v, p3o, p3x)
        STAMPW(13, 0);
        float y = sw16_add(sw32_add(v[0], v[2]), sw32_add(v[1], v[3]));
        y += dpp<ROR8>(y); y += dpp<ROR4>(y);
        if (lc < 4) *(LAS float*)(lds + L_R3 + (4 * lc + lg) * 16 + w * 4) = y;
        BAR();
        if (t < 16) {
          const f32x4 rs = *(LAS f32x4*)(lds + L_R3 + t * 16);
          gout[oc] = __builtin_bit_cast(u16, (__bf16)(xo + rbf(gbv * rbf((rs[0] + rs[1]) + (rs[2] + rs[3])))));
          if (b == 0 && t == 0) ST32(tag, rws, WS_EPOCH, 0, AUX_SC1);
        }
        STAMPW(14, 0);
      }
    }
    KEEP();
  }
}

extern "C" void kernel_launch(void* const* d_in, const int* in_sizes, int n_in, void* d_out, int out_size, void* d_ws, size_t ws_size, hipStream_t stream) {
  for (int i = 0; i < n_in; ++i) fprintf(stderr, "in%d %p\n", i, d_in[i]); fprintf(stderr, "out %p ws %p\n", d_out, d_ws);
  { hipPointerAttribute_t at; if (hipPointerGetAttributes(&at, d_ws) == hipSuccess) fprintf(stderr, "ws type %d flags %u managed %d\n", (int)at.type, at.allocationFlags, at.isManaged); if (hipPointerGetAttributes(&at, d_in[1]) == hipSuccess) fprintf(stderr, "K type %d flags %u\n", (int)at.type, at.allocationFlags); }
  (void)hipFuncSetAttribute((const void*)fused_block, hipFuncAttributeMaxDynamicSharedMemorySize, L_END);
  fused_block<<<256, 768, L_END, stream>>>((const u16*)d_in[0], (const u16*)d_in[1], (const u16*)d_in[2], (const u16*)d_in[3], (const u16*)d_in[4],
                                           (const u16*)d_in[6], (const u16*)d_in[7], (const u16*)d_in[8], (const u16*)d_in[9], (const u16*)d_in[10],
                                           (u16*)d_out, (unsigned char*)d_ws);
}
```

```cpp
#define PG 256
#define PT 768
#define PL 155000
#define FIXT 4000
#include <hip/hip_runtime.h>
#include <hip/hip_bf16.h>

typedef unsigned short u16;
typedef unsigned int u32;
typedef unsigned long long u64;
typedef u32 u32x2 __attribute__((ext_vector_type(2)));
typedef u32 u32x4 __attribute__((ext_vector_type(4)));
typedef float f32x2 __attribute__((ext_vector_type(2)));
typedef float f32x4 __attribute__((ext_vector_type(4)));
typedef short s16x4 __attribute__((ext_vector_type(4)));
typedef __bf16 bf16x2 __attribute__((ext_vector_type(2)));
typedef __bf16 bf16x8 __attribute__((ext_vector_type(8)));
typedef __amdgpu_buffer_rsrc_t rsrc_t;
#define LAS __attribute__((address_space(3)))

#define STAMP(k) do {} while (0)
#define STAMPW(k, wv) do {} while (0)
#define DBGCNT(k, v) do {} while (0)
#define ABL 0
#define WIN 4
#define QPOS 12
#define WARM_AT 9
#define POLL_GAP 1
#define POLL_SLOW 12
#define POLL_HI 160
#define P1_HI 24
#define AUX_WARM 0
#define QG_DELAY 4

constexpr int D = 512, SK = 16384, DH = 128, FF = 1792;
constexpr u32 SENT = 0xFFFFFFFFu;
constexpr int AUX_NT = 2, AUX_SC1 = 16;
constexpr int SPIN_CAP = 1 << 16;
constexpr int WS_EPOCH = 0;
constexpr int WS_RDY = 1024;
constexpr int WS_MG = 4096;
constexpr int WS_QG = WS_MG + 1536 * 8;
constexpr int WS_X1 = WS_QG + 16384;
constexpr int WS_PW = WS_X1 + 2048;
constexpr int WS_PO = WS_PW + 65536;
constexpr int WS_PD = WS_PO + 1048576;
constexpr int WS_END = WS_PD + 16 * 224 * 128;
constexpr int OOB = 0x40000000;
constexpr int L_K = 0;
constexpr int L_ACC = 131072;
constexpr int L_Q = L_ACC + 16384;
constexpr int L_H = L_Q + 1024;
constexpr int L_M = L_H + 1024;
constexpr int L_L = L_M + 128;
constexpr int L_R1 = L_L + 128;
constexpr int L_XS = L_R1 + 2048;
constexpr int L_ST = L_XS + 2048;
constexpr int L_R2 = L_ST + 128;
constexpr int L_R3 = L_R2 + 256;
constexpr int L_FM = L_R3 + 256;
constexpr int L_FLAG = L_FM + 512;
constexpr int F_CQ = 0, F_QRDY = 4, F_RDY = 8, F_ISS = 12, F_ATT = 16, F_WARM = 20, F_SM = 24;
constexpr int L_STAMP = L_FLAG + 64;
constexpr int L_END = L_STAMP + 256;
constexpr float L2E = 1.4426950408889634f;

__device__ __forceinline__ float bfl(u32 x) { return __uint_as_float(x << 16); }
__device__ __forceinline__ float bfh(u32 x) { return __uint_as_float(x & 0xffff0000u); }
__device__ __forceinline__ u32 pk2(float lo, float hi) { bf16x2 v = {(__bf16)lo, (__bf16)hi}; return __builtin_bit_cast(u32, v); }
__device__ __forceinline__ float rbf(float f) { return (float)(__bf16)f; }
__device__ __forceinline__ float fmx(float a, float b) { return __builtin_elementwise_maximum(a, b); }
template <int C> __device__ __forceinline__ u32 dppu(u32 x) { return (u32)__builtin_amdgcn_mov_dpp((int)x, C, 0xf, 0xf, true); }
template <int C> __device__ __forceinline__ float dpp(float x) { return __uint_as_float(dppu<C>(__float_as_uint(x))); }
constexpr int XOR1 = 0xB1, XOR2 = 0x4E, ROR4 = 0x124, ROR8 = 0x128;
__device__ __forceinline__ float sw16_add(float a, float b) {
  auto r = __builtin_amdgcn_permlane16_swap(__float_as_uint(a), __float_as_uint(b), false, false);
  return __uint_as_float(r[0]) + __uint_as_float(r[1]);
}
__device__ __forceinline__ float sw32_add(float a, float b) {
  auto r = __builtin_amdgcn_permlane32_swap(__float_as_uint(a), __float_as_uint(b), false, false);
  return __uint_as_float(r[0]) + __uint_as_float(r[1]);
}
__device__ __forceinline__ float sw16_max(float a) {
  auto r = __builtin_amdgcn_permlane16_swap(__float_as_uint(a), __float_as_uint(a), false, false);
  return fmx(__uint_as_float(r[0]), __uint_as_float(r[1]));
}
__device__ __forceinline__ float sw32_max(float a) {
  auto r = __builtin_amdgcn_permlane32_swap(__float_as_uint(a), __float_as_uint(a), false, false);
  return fmx(__uint_as_float(r[0]), __uint_as_float(r[1]));
}
__device__ __forceinline__ float wave_sum2(float a, float b) {
  float x = sw32_add(a, b);
  x = sw16_add(x, x);
  x += dpp<XOR1>(x); x += dpp<XOR2>(x); x += dpp<ROR8>(x); x += dpp<ROR4>(x);
  return x;
}
__device__ __forceinline__ rsrc_t mk(const void* p, u32 bytes) { return __builtin_amdgcn_make_buffer_rsrc((void*)p, 0, bytes, 0x00020000); }
#define LD128(r, v, s, a) __builtin_amdgcn_raw_buffer_load_b128(r, v, s, a)
#define LD64(r, v, s, a) __builtin_amdgcn_raw_buffer_load_b64(r, v, s, a)
#define LD32(r, v, s, a) __builtin_amdgcn_raw_buffer_load_b32(r, v, s, a)
#define ST128(d, r, v, s, a) __builtin_amdgcn_raw_buffer_store_b128(d, r, v, s, a)
#define ST64(d, r, v, s, a) __builtin_amdgcn_raw_buffer_store_b64(d, r, v, s, a)
#define ST32(d, r, v, s, a) __builtin_amdgcn_raw_buffer_store_b32(d, r, v, s, a)
__device__ __forceinline__ u32 canon(float f) { return min(__float_as_uint(f), 0xFFFFFFFEu); }
__device__ __forceinline__ u32x4 canon4(f32x4 v) { return u32x4{canon(v[0]), canon(v[1]), canon(v[2]), canon(v[3])}; }
__device__ __forceinline__ u32 max4(u32x4 v) { return max(max(v[0], v[1]), max(v[2], v[3])); }
__device__ __forceinline__ f32x4 asf(u32x4 v) { return __builtin_bit_cast(f32x4, v); }
__device__ __forceinline__ float rcp(float x) { return __builtin_amdgcn_rcpf(x); }
__device__ __forceinline__ float silu(float x) { return x * rcp(1.f + __expf(-x)); }
__device__ __forceinline__ float dot2(u32 a, u32 b, float c) { return __builtin_amdgcn_fdot2_f32_bf16(__builtin_bit_cast(bf16x2, a), __builtin_bit_cast(bf16x2, b), c, false); }
#define MEMF() asm volatile("" ::: "memory")
#define LGKM0() asm volatile("s_waitcnt lgkmcnt(0)" ::: "memory")
#define VM0() asm volatile("s_waitcnt vmcnt(0)" ::: "memory")
#define BAR() do { LGKM0(); __builtin_amdgcn_s_barrier(); MEMF(); } while (0)
#define SB() __builtin_amdgcn_sched_barrier(0)
#define PACE() asm volatile("s_waitcnt vmcnt(%0)" :: "i"(WIN - 1) : "memory")
#define PIN(x) asm volatile("" : "+v"(x))
__device__ __forceinline__ u32 lds_inc(LAS unsigned char* p) { return __hip_atomic_fetch_add((LAS u32*)p, 1u, __ATOMIC_RELAXED, __HIP_MEMORY_SCOPE_WORKGROUP); }
__device__ __forceinline__ void lds_wait_ge(LAS unsigned char* p, u32 n) {
#pragma nounroll
  for (int sp = 0; sp < SPIN_CAP * 16; ++sp) { if (*(volatile LAS u32*)p >= n) break; __builtin_amdgcn_s_sleep(1); }
  MEMF();
}
__device__ __forceinline__ void ln_fin(float c, float S1, float S2, float& m, float& den) {
  const float d = S1 * (1.f / D);
  const float var = rbf(fmaxf(fmaf(-d, d, S2 * (1.f / D)), 0.f));
  m = rbf(c + d); den = rcp(rbf(__builtin_amdgcn_sqrtf(rbf(var + 1e-5f))));
}
__device__ __forceinline__ void gemv32(const u32 (&wp)[32], const u32 (&hp)[4], float& r0, float& r1) {
  float a[8];
#pragma unroll
  for (int j = 0; j < 8; ++j) a[j] = dot2(wp[24 + j], hp[3], dot2(wp[16 + j], hp[2], dot2(wp[8 + j], hp[1], dot2(wp[j], hp[0], 0.f))));
#pragma unroll
  for (int j = 0; j < 4; ++j) a[j] = sw32_add(a[j], a[j + 4]);
#pragma unroll
  for (int j = 0; j < 2; ++j) a[j] = sw16_add(a[j], a[j + 2]);
#pragma unroll
  for (int j = 0; j < 2; ++j) { a[j] += dpp<ROR8>(a[j]); a[j] += dpp<ROR4>(a[j]); }
  r0 = a[0]; r1 = a[1];
}
#define PAIR_LO(hi, lo) __builtin_amdgcn_perm(hi, lo, 0x05040100u)
#define PAIR_HI(hi, lo) __builtin_amdgcn_perm(hi, lo, 0x07060302u)
#define PH_ISSUE(vo_, so_) asm volatile("s_nop 4\n\t" \
               "buffer_load_dwordx4 v[100:103], %[vo], %[rs], %[so] offen sc1\n\t" \
                : "+{v[100:103]}"(P[0]) : [vo] "v"(vo_), [so] "s"(so_), [rs] "s"(rws) : "memory")
#define PH_WAIT(n) asm volatile("s_waitcnt vmcnt(" #n ")" : "+{v[100:103]}"(P[0]) :: "memory")
#define PH_TIE() asm volatile("" : "+{v[100:103]}"(P[0]))
#define PQ_ISSUE(vo_) asm volatile("s_nop 4\n\t" \
               "buffer_load_dwordx4 v[100:103], %[vo], %[rs], 0 offen sc1\n\t" \
               "buffer_load_dwordx4 v[104:107], %[vo], %[rs], 0 offen offset:1024 sc1\n\t" \
                : "+{v[100:103]}"(P[0]), "+{v[104:107]}"(P[1]) : [vo] "v"(vo_), [rs] "s"(rws) : "memory")
#define PQ_WAIT(n) asm volatile("s_waitcnt vmcnt(" #n ")" : "+{v[100:103]}"(P[0]), "+{v[104:107]}"(P[1]) :: "memory")
#define PQ_TIE() asm volatile("" : "+{v[100:103]}"(P[0]), "+{v[104:107]}"(P[1]))
#define PM_ISSUE(vm_) asm volatile("s_nop 4\n\t" \
               "buffer_load_dwordx4 v[116:119], %[vm], %[rs], 0 offen sc1\n\t" \
                : "+{v[116:119]}"(P[4]) : [vm] "v"(vm_), [rs] "s"(rws) : "memory")
#define PM_WAIT(n) asm volatile("s_waitcnt vmcnt(" #n ")" : "+{v[116:119]}"(P[4]) :: "memory")
#define PM_TIE() asm volatile("" : "+{v[116:119]}"(P[4]))
#define P1_ISSUE(vo_) asm volatile("s_nop 4\n\t" \
               "buffer_load_dwordx4 v[100:103], %[vo], %[rs], 0 offen sc1\n\t" \
               "buffer_load_dwordx4 v[104:107], %[vo], %[rs], 0 offen offset:2048 sc1\n\t" \
               "buffer_load_dwordx4 v[108:111], %[vo], %[rs], %[s1] offen sc1\n\t" \
               "buffer_load_dwordx4 v[112:115], %[vo], %[rs], %[s1] offen offset:2048 sc1\n\t" \
                : "+{v[100:103]}"(P[0]), "+{v[104:107]}"(P[1]), "+{v[108:111]}"(P[2]), "+{v[112:115]}"(P[3]) : [vo] "v"(vo_), [s1] "s"(4096), [rs] "s"(rws) : "memory")
#define P1_WAIT(n) asm volatile("s_waitcnt vmcnt(" #n ")" : "+{v[100:103]}"(P[0]), "+{v[104:107]}"(P[1]), "+{v[108:111]}"(P[2]), "+{v[112:115]}"(P[3]) :: "memory")
#define P1_TIE() asm volatile("" : "+{v[100:103]}"(P[0]), "+{v[104:107]}"(P[1]), "+{v[108:111]}"(P[2]), "+{v[112:115]}"(P[3]))
#define PX_ISSUE(vo_) asm volatile("s_nop 4\n\t" \
               "buffer_load_dwordx4 v[100:103], %[vo], %[rs], 0 offen sc1\n\t" \
               "buffer_load_dwordx4 v[104:107], %[vo], %[rs], 0 offen offset:1024 sc1\n\t" \
                : "+{v[100:103]}"(P[0]), "+{v[104:107]}"(P[1]) : [vo] "v"(vo_), [rs] "s"(rws) : "memory")
#define PX_WAIT(n) asm volatile("s_waitcnt vmcnt(" #n ")" : "+{v[100:103]}"(P[0]), "+{v[104:107]}"(P[1]) :: "memory")
#define PX_TIE() asm volatile("" : "+{v[100:103]}"(P[0]), "+{v[104:107]}"(P[1]))
#define P2_SUMS(r0_, vo_) { int cnt_ = SPIN_CAP / 4, np_, tt_; f32x4 t0_, t1_; asm volatile( \
      "s_nop 4\n\t" \
      "4:\n\t" \
      "buffer_load_dwordx4 v[100:103], %[vo], %[rs], 0 offen sc1\n\t" \
      "buffer_load_dwordx4 v[104:107], %[vo], %[rs], 0 offen offset:1024 sc1\n\t" \
      "buffer_load_dwordx4 v[108:111], %[vo], %[rs], 0 offen offset:2048 sc1\n\t" \
      "buffer_load_dwordx4 v[112:115], %[vo], %[rs], 0 offen offset:3072 sc1\n\t" \
      "1:\n\t" \
      "s_waitcnt vmcnt(0)\n\t" \
      "v_pk_add_f32 v[92:93], v[100:101], v[104:105]\n\t" \
      "v_pk_add_f32 v[94:95], v[102:103], v[106:107]\n\t" \
      "v_pk_add_f32 v[84:85], v[108:109], v[112:113]\n\t" \
      "v_pk_add_f32 v[86:87], v[110:111], v[114:115]\n\t" \
      "v_pk_add_f32 v[92:93], v[92:93], v[84:85]\n\t" \
      "v_pk_add_f32 v[94:95], v[94:95], v[86:87]\n\t" \
      "s_nop 0\n\t" \
      "v_pk_add_f32 v[84:85], v[92:93], v[94:95]\n\t" \
      "s_nop 0\n\t" \
      "v_cmp_u_f32 vcc, v84, v85\n\t" \
      "s_cbranch_vccz 2f\n\t" \
      "s_sub_u32 %[cnt], %[cnt], 1\n\t" \
      "s_cbranch_scc1 2f\n\t" \
      "s_mov_b32 %[np], 0\n\t" \
      "v_cmp_eq_u32 vcc, -1, v100\n\t" \
      "s_bcnt1_i32_b64 %[t], vcc\n\t" \
      "s_add_u32 %[np], %[np], %[t]\n\t" \
      "v_cmp_eq_u32 vcc, -1, v104\n\t" \
      "s_bcnt1_i32_b64 %[t], vcc\n\t" \
      "s_add_u32 %[np], %[np], %[t]\n\t" \
      "v_cmp_eq_u32 vcc, -1, v108\n\t" \
      "s_bcnt1_i32_b64 %[t], vcc\n\t" \
      "s_add_u32 %[np], %[np], %[t]\n\t" \
      "v_cmp_eq_u32 vcc, -1, v112\n\t" \
      "s_bcnt1_i32_b64 %[t], vcc\n\t" \
      "s_add_u32 %[np], %[np], %[t]\n\t" \
      "s_cmp_eq_u32 %[np], 0\n\t" \
      "s_cbranch_scc1 4b\n\t" \
      "s_cmp_gt_u32 %[np], %[hi]\n\t" \
      "s_cbranch_scc0 3f\n\t" \
      "s_sleep %[slow]\n\t" \
      "3:\n\t" \
      "s_sleep %[gap]\n\t" \
      "v_cmp_eq_u32 vcc, -1, v100\n\t" \
      "s_mov_b64 exec, vcc\n\t" \
      "s_cbranch_execz 5f\n\t" \
      "buffer_load_dwordx4 v[100:103], %[vo], %[rs], 0 offen sc1\n\t" \
      "5:\n\t" \
      "s_mov_b64 exec, -1\n\t" \
      "v_cmp_eq_u32 vcc, -1, v104\n\t" \
      "s_mov_b64 exec, vcc\n\t" \
      "s_cbranch_execz 5f\n\t" \
      "buffer_load_dwordx4 v[104:107], %[vo], %[rs], 0 offen offset:1024 sc1\n\t" \
      "5:\n\t" \
      "s_mov_b64 exec, -1\n\t" \
      "v_cmp_eq_u32 vcc, -1, v108\n\t" \
      "s_mov_b64 exec, vcc\n\t" \
      "s_cbranch_execz 5f\n\t" \
      "buffer_load_dwordx4 v[108:111], %[vo], %[rs], 0 offen offset:2048 sc1\n\t" \
      "5:\n\t" \
      "s_mov_b64 exec, -1\n\t" \
      "v_cmp_eq_u32 vcc, -1, v112\n\t" \
      "s_mov_b64 exec, vcc\n\t" \
      "s_cbranch_execz 5f\n\t" \
      "buffer_load_dwordx4 v[112:115], %[vo], %[rs], 0 offen offset:3072 sc1\n\t" \
      "5:\n\t" \
      "s_mov_b64 exec, -1\n\t" \
      "s_branch 1b\n\t" \
      "2:\n\t" "s_nop 0" \
      : "+{v[100:103]}"(P[0]), "+{v[104:107]}"(P[1]), "+{v[108:111]}"(P[2]), "+{v[112:115]}"(P[3]), "=&{v[92:95]}"(r0_), "=&{v[84:87]}"(t0_), "=&{v[88:91]}"(t1_), [cnt] "+&s"(cnt_), [np] "=&s"(np_), [t] "=&s"(tt_) \
      : [vo] "v"(vo_), [rs] "s"(rws), [gap] "i"(POLL_GAP), [slow] "i"(POLL_SLOW), [hi] "i"(POLL_HI) : "vcc", "scc", "memory"); DBGCNT(21, SPIN_CAP / 4 - cnt_ + 1); }
#define P3_SUMS(r0_, vo_, v3_) { int cnt_ = SPIN_CAP / 4, np_, tt_; f32x4 t0_, t1_; asm volatile( \
      "s_nop 4\n\t" \
      "4:\n\t" \
      "buffer_load_dwordx4 v[100:103], %[vo], %[rs], 0 offen sc1\n\t" \
      "buffer_load_dwordx4 v[104:107], %[vo], %[rs], %[s1] offen sc1\n\t" \
      "buffer_load_dwordx4 v[108:111], %[vo], %[rs], %[s2] offen sc1\n\t" \
      "buffer_load_dwordx4 v[112:115], %[v3], %[rs], 0 offen sc1\n\t" \
      "1:\n\t" \
      "s_waitcnt vmcnt(0)\n\t" \
      "v_pk_add_f32 v[92:93], v[100:101], v[104:105]\n\t" \
      "v_pk_add_f32 v[94:95], v[102:103], v[106:107]\n\t" \
      "v_pk_add_f32 v[84:85], v[108:109], v[112:113]\n\t" \
      "v_pk_add_f32 v[86:87], v[110:111], v[114:115]\n\t" \
      "v_pk_add_f32 v[92:93], v[92:93], v[84:85]\n\t" \
      "v_pk_add_f32 v[94:95], v[94:95], v[86:87]\n\t" \
      "s_nop 0\n\t" \
      "v_pk_add_f32 v[84:85], v[92:93], v[94:95]\n\t" \
      "s_nop 0\n\t" \
      "v_cmp_u_f32 vcc, v84, v85\n\t" \
      "s_cbranch_vccz 2f\n\t" \
      "s_sub_u32 %[cnt], %[cnt], 1\n\t" \
      "s_cbranch_scc1 2f\n\t" \
      "s_mov_b32 %[np], 0\n\t" \
      "v_cmp_eq_u32 vcc, -1, v100\n\t" \
      "s_bcnt1_i32_b64 %[t], vcc\n\t" \
      "s_add_u32 %[np], %[np], %[t]\n\t" \
      "v_cmp_eq_u32 vcc, -1, v104\n\t" \
      "s_bcnt1_i32_b64 %[t], vcc\n\t" \
      "s_add_u32 %[np], %[np], %[t]\n\t" \
      "v_cmp_eq_u32 vcc, -1, v108\n\t" \
      "s_bcnt1_i32_b64 %[t], vcc\n\t" \
      "s_add_u32 %[np], %[np], %[t]\n\t" \
      "v_cmp_eq_u32 vcc, -1, v112\n\t" \
      "s_bcnt1_i32_b64 %[t], vcc\n\t" \
      "s_add_u32 %[np], %[np], %[t]\n\t" \
      "s_cmp_eq_u32 %[np], 0\n\t" \
      "s_cbranch_scc1 4b\n\t" \
      "s_cmp_gt_u32 %[np], %[hi]\n\t" \
      "s_cbranch_scc0 3f\n\t" \
      "s_sleep %[slow]\n\t" \
      "3:\n\t" \
      "s_sleep %[gap]\n\t" \
      "v_cmp_eq_u32 vcc, -1, v100\n\t" \
      "s_mov_b64 exec, vcc\n\t" \
      "s_cbranch_execz 5f\n\t" \
      "buffer_load_dwordx4 v[100:103], %[vo], %[rs], 0 offen sc1\n\t" \
      "5:\n\t" \
      "s_mov_b64 exec, -1\n\t" \
      "v_cmp_eq_u32 vcc, -1, v104\n\t" \
      "s_mov_b64 exec, vcc\n\t" \
      "s_cbranch_execz 5f\n\t" \
      "buffer_load_dwordx4 v[104:107], %[vo], %[rs], %[s1] offen sc1\n\t" \
      "5:\n\t" \
      "s_mov_b64 exec, -1\n\t" \
      "v_cmp_eq_u32 vcc, -1, v108\n\t" \
      "s_mov_b64 exec, vcc\n\t" \
      "s_cbranch_execz 5f\n\t" \
      "buffer_load_dwordx4 v[108:111], %[vo], %[rs], %[s2] offen sc1\n\t" \
      "5:\n\t" \
      "s_mov_b64 exec, -1\n\t" \
      "v_cmp_eq_u32 vcc, -1, v112\n\t" \
      "s_mov_b64 exec, vcc\n\t" \
      "s_cbranch_execz 5f\n\t" \
      "buffer_load_dwordx4 v[112:115], %[v3], %[rs], 0 offen sc1\n\t" \
      "5:\n\t" \
      "s_mov_b64 exec, -1\n\t" \
      "s_branch 1b\n\t" \
      "2:\n\t" "s_nop 0" \
      : "+{v[100:103]}"(P[0]), "+{v[104:107]}"(P[1]), "+{v[108:111]}"(P[2]), "+{v[112:115]}"(P[3]), "=&{v[92:95]}"(r0_), "=&{v[84:87]}"(t0_), "=&{v[88:91]}"(t1_), [cnt] "+&s"(cnt_), [np] "=&s"(np_), [t] "=&s"(tt_) \
      : [vo] "v"(vo_), [v3] "v"(v3_), [s1] "s"(8192), [s2] "s"(16384), [rs] "s"(rws), [gap] "i"(POLL_GAP), [slow] "i"(POLL_SLOW), [hi] "i"(POLL_HI) : "vcc", "scc", "memory"); DBGCNT(16, SPIN_CAP / 4 - cnt_ + 1); }
#define KEEP() asm volatile("; keep" :: "{v[100:103]}"(P[0]), "{v[104:107]}"(P[1]), "{v[108:111]}"(P[2]), "{v[112:115]}"(P[3]), "{v[116:119]}"(P[4]), "{v[120:123]}"(P[5]))
#define POOL_INIT() asm volatile("; pool" : "={v[100:103]}"(P[0]), "={v[104:107]}"(P[1]), "={v[108:111]}"(P[2]), "={v[112:115]}"(P[3]), "={v[116:119]}"(P[4]), "={v[120:123]}"(P[5]))

#define POLL(NAME, N, PEND, ...)                     \
  NAME##_ISSUE(__VA_ARGS__); NAME##_WAIT(0);                                                      \
  { bool pend_ = PEND;                                                                            \
    if (__any(pend_)) {                                                                           \
      if (pend_) NAME##_ISSUE(__VA_ARGS__);                                                       \
      __builtin_amdgcn_s_sleep(4);                                                                \
      _Pragma("nounroll") for (int sp = 0; sp < SPIN_CAP; ++sp) {                                 \
        if (pend_) NAME##_ISSUE(__VA_ARGS__);                                                     \
        NAME##_WAIT(N); pend_ = PEND;                                                             \
        if (!__any(pend_)) break;                                                                 \
      } }                                                                                         \
    NAME##_TIE(); }
#define POLLB(NAME, HI, PEND, ...)     \
  NAME##_ISSUE(__VA_ARGS__);                                                                      \
  _Pragma("nounroll") for (int sp = 0; sp < SPIN_CAP; ++sp) {                                     \
    NAME##_WAIT(0);                                                                               \
    DBGCNT(20, sp + 1);                                                                           \
    const bool pend_ = PEND;                                                                      \
    const u64 bal_ = __ballot(pend_);                                                             \
    if (bal_ == 0) break;                                                                         \
    if (__popcll(bal_) > HI) __builtin_amdgcn_s_sleep(POLL_SLOW);                                 \
    __builtin_amdgcn_s_sleep(POLL_GAP);                                                           \
    if (pend_) NAME##_ISSUE(__VA_ARGS__);                                                         \
  }

__global__ void __launch_bounds__(768, 3)
fused_block(const u16* __restrict__ gx, const u16* __restrict__ gk, const u16* __restrict__ gv, const u16* __restrict__ gwq,
            const u16* __restrict__ gwo, const u16* __restrict__ gwg, const u16* __restrict__ gwu, const u16* __restrict__ gwd,
            const u16* __restrict__ gc, const u16* __restrict__ gwm, u16* __restrict__ gout, unsigned char* __restrict__ gws) {
  extern __shared__ __attribute__((aligned(16))) unsigned char smem[];
  LAS unsigned char* lds = (LAS unsigned char*)smem;
  const int b = blockIdx.x, g = b & 7, s = b >> 3;
  const int t = threadIdx.x, w = __builtin_amdgcn_readfirstlane(t >> 6), l = t & 63, lg = l >> 4, lc = l & 15;
  const rsrc_t rws = mk(gws, WS_END);
  const u64 tst_ = __builtin_amdgcn_s_memrealtime();
  STAMPW(0, 0);
  asm volatile("" :: "s"(gx), "s"(gk), "s"(gv), "s"(gwq), "s"(gwo), "s"(gwg), "s"(gwu), "s"(gwd), "s"(gc), "s"(gwm), "s"(gout), "s"(gws));
  if (__builtin_expect(w >= 4, 1)) {
    const int a = w - 4;
    const size_t kvo = ((size_t)g * SK + s * 512) * DH;
    const rsrc_t rk = mk(gk + kvo, (ABL & 256) ? 0 : 131072), rv = mk(gv + kvo, (ABL & 256) ? 0 : 131072);
    {
      const int v0 = lg * 256 + ((lc ^ (4 * lg + 0)) * 16), v1 = lg * 256 + ((lc ^ (4 * lg + 1)) * 16);
      const int v2 = lg * 256 + ((lc ^ (4 * lg + 2)) * 16), v3 = lg * 256 + ((lc ^ (4 * lg + 3)) * 16);
#pragma unroll
      for (int T = 0; T < 4; ++T) {
        const int so = a * 16384 + T * 4096;
        if (T == 0)
          asm volatile("s_mov_b32 m0, %5\n\ts_nop 4\n\t"
                       "buffer_load_dwordx4 %0, %4, %5 offen nt lds\n\t"
                       "buffer_load_dwordx4 %1, %4, %5 offen offset:1024 nt lds\n\t"
                       "s_barrier\n\t"
                       "s_waitcnt vmcnt(%6)\n\tbuffer_load_dwordx4 %2, %4, %5 offen offset:2048 nt lds\n\t"
                       "s_waitcnt vmcnt(%6)\n\tbuffer_load_dwordx4 %3, %4, %5 offen offset:3072 nt lds"
                       :: "v"(v0), "v"(v1), "v"(v2), "v"(v3), "s"(rk), "s"(so), "i"(WIN - 1) : "memory");
        else
          asm volatile("s_mov_b32 m0, %5\n\ts_nop 4\n\t"
                       "s_waitcnt vmcnt(%6)\n\tbuffer_load_dwordx4 %0, %4, %5 offen nt lds\n\t"
                       "s_waitcnt vmcnt(%6)\n\tbuffer_load_dwordx4 %1, %4, %5 offen offset:1024 nt lds\n\t"
                       "s_waitcnt vmcnt(%6)\n\tbuffer_load_dwordx4 %2, %4, %5 offen offset:2048 nt lds\n\t"
                       "s_waitcnt vmcnt(%6)\n\tbuffer_load_dwordx4 %3, %4, %5 offen offset:3072 nt lds"
                       :: "v"(v0), "v"(v1), "v"(v2), "v"(v3), "s"(rk), "s"(so), "i"(WIN - 1) : "memory");
      }
    }
    u32x4 vf[16];
#define VLOAD(i) vf[i] = LD128(rv, l * 16 + ((i) & 3) * 1024, a * 16384 + ((i) >> 2) * 4096, AUX_NT)
#pragma unroll
    for (int i = 0; i < QPOS; ++i) { PACE(); VLOAD(i); if (i == WARM_AT && l == 0) lds_inc(lds + L_FLAG + F_WARM); }
    SB();
    const u32 hm = (lc & 1) ? 0xffff0000u : 0x0000ffffu;
    u32 m0 = (lc & 2) ? 0u : hm, m1 = (lc & 2) ? hm : 0u;
    int ka[4], pubo = WS_PO + (((4 * g + (a & 3)) * 8 + (l >> 3)) * 32 + s) * 128 + (l & 7) * 16;
#pragma unroll
    for (int tt = 0; tt < 4; ++tt) { ka[tt] = L_K + a * 16384 + (4 * (lc & 3) + (lc >> 2)) * 256 + (((4 * tt + lg) ^ lc) * 16); PIN(ka[tt]); }
    PIN(m0); PIN(m1); PIN(pubo);
    STAMPW(5, 4);
    lds_wait_ge(lds + L_FLAG + F_QRDY, 1u);
#pragma unroll
    for (int i = QPOS; i < 16; ++i) VLOAD(i);
    SB();
    if (l == 0) lds_inc(lds + L_FLAG + F_ISS);
    u32x4 qf[4];
#pragma unroll
    for (int tt = 0; tt < 4; ++tt) qf[tt] = *(LAS u32x4*)(lds + L_Q + (lc >> 2) * 256 + tt * 64 + lg * 16);
    asm volatile("s_waitcnt vmcnt(16)" ::: "memory");
    STAMPW(6, 4);
    f32x4 sc;
    {
      f32x4 c[4];
#pragma unroll
      for (int T = 0; T < 4; ++T) {
        c[T] = f32x4{0.f, 0.f, 0.f, 0.f};
#pragma unroll
        for (int tt = 0; tt < 4; ++tt)
          c[T] = __builtin_amdgcn_mfma_f32_16x16x32_bf16(__builtin_bit_cast(bf16x8, *(LAS u32x4*)(lds + ka[tt] + T * 4096)), __builtin_bit_cast(bf16x8, qf[tt]), c[T], 0, 0, 0);
      }
      const f32x4 lo = (l & 1) ? c[1] : c[0], hi = (l & 1) ? c[3] : c[2];
      sc = (l & 2) ? hi : lo;
    }
#pragma unroll
    for (int r = 0; r < 4; ++r) sc[r] = rbf(rbf(sc[r]) * 0.08838834764831845f);
    float mw = fmx(fmx(sc[0], sc[1]), fmx(sc[2], sc[3]));
    mw = fmx(mw, dpp<XOR1>(mw)); mw = fmx(mw, dpp<XOR2>(mw)); mw = sw32_max(sw16_max(mw));
    u32 pp[4];
    float lw = 0.f;
#pragma unroll
    for (int r = 0; r < 4; ++r) { const float p = __builtin_amdgcn_exp2f((sc[r] - mw) * L2E); lw += p; pp[r] = pk2(p, p); }
    f32x4 o0 = {0.f, 0.f, 0.f, 0.f}, o1 = {0.f, 0.f, 0.f, 0.f};
#define PV(i, Q) { const u32 x_ = dppu<Q>(pp[(i) & 3]); const u32x2 pa_ = {x_ & m0, x_ & m1};                                                         \
      o0 = __builtin_amdgcn_mfma_f32_16x16x16bf16_1k(__builtin_bit_cast(s16x4, pa_), __builtin_bit_cast(s16x4, u32x2{vf[i][0], vf[i][1]}), o0, 0, 0, 0); \
      o1 = __builtin_amdgcn_mfma_f32_16x16x16bf16_1k(__builtin_bit_cast(s16x4, pa_), __builtin_bit_cast(s16x4, u32x2{vf[i][2], vf[i][3]}), o1, 0, 0, 0); }
    PV(0, 0x00) PV(1, 0x00) PV(2, 0x00) PV(3, 0x00)
    lw += dpp<XOR1>(lw); lw += dpp<XOR2>(lw); lw = sw16_add(lw, lw); lw = sw32_add(lw, lw);
    if (lg == 0 && (lc & 3) == 0) { *(LAS float*)(lds + L_M + (lc >> 2) * 32 + a * 4) = mw; *(LAS float*)(lds + L_L + (lc >> 2) * 32 + a * 4) = lw; }
    LGKM0();
    if (l == 0) lds_inc(lds + L_FLAG + F_SM);
    PV(4, 0x55) PV(5, 0x55) PV(6, 0x55) PV(7, 0x55) PV(8, 0xAA) PV(9, 0xAA) PV(10, 0xAA) PV(11, 0xAA)
    float e[8], M = 0.f, L = 0.f;
    if (a < 4) {
      lds_wait_ge(lds + L_FLAG + F_SM, 8u);
      const f32x4 ma = *(LAS f32x4*)(lds + L_M + a * 32), mb = *(LAS f32x4*)(lds + L_M + a * 32 + 16);
      const f32x4 la = *(LAS f32x4*)(lds + L_L + a * 32), lb = *(LAS f32x4*)(lds + L_L + a * 32 + 16);
      M = fmx(fmx(fmx(ma[0], ma[1]), fmx(ma[2], ma[3])), fmx(fmx(mb[0], mb[1]), fmx(mb[2], mb[3])));
#pragma unroll
      for (int i = 0; i < 8; ++i) {
        e[i] = __builtin_amdgcn_exp2f(((i < 4 ? ma[i & 3] : mb[i & 3]) - M) * L2E);
        L = fmaf(e[i], i < 4 ? la[i & 3] : lb[i & 3], L);
        PIN(e[i]);
      }
      PIN(L);
    }
    SB();
    PV(12, 0xFF) PV(13, 0xFF) PV(14, 0xFF) PV(15, 0xFF)
    __builtin_amdgcn_s_setprio(3);
    *(LAS f32x4*)(lds + L_ACC + a * 2048 + lg * 512 + lc * 32) = o0;
    *(LAS f32x4*)(lds + L_ACC + a * 2048 + lg * 512 + lc * 32 + 16) = o1;
    LGKM0();
    if (l == 0) lds_inc(lds + L_FLAG + F_ATT);
    STAMPW(7, 11); STAMPW(23, 4);
    if (a < 4) {
      const int ao = L_ACC + a * 512 + ((l >> 3) * 4 + (l & 3)) * 16;
      lds_wait_ge(lds + L_FLAG + F_ATT, 8u);
      f32x4 oa = e[0] * *(LAS f32x4*)(lds + ao), ob = e[1] * *(LAS f32x4*)(lds + ao + 2048);
#pragma unroll
      for (int i = 2; i < 8; i += 2) { oa += e[i] * *(LAS f32x4*)(lds + ao + i * 2048); ob += e[i + 1] * *(LAS f32x4*)(lds + ao + (i + 1) * 2048); }
      f32x4 o = oa + ob;
      if (l & 4) o = f32x4{M, L, 0.f, 0.f};
      ST128(canon4(o), rws, pubo, 0, AUX_SC1);
      STAMPW(8, 4);
    }
  } else {
    const rsrc_t rx = mk(gx, 1024);
    const int hh = 4 * g + (s >> 3), cs = s & 7, uj = 28 * g + s, fin = 4 * g + s;
    const bool ffn = s < 28, red = s == 28 || s == 29;
    const int rid = 2 * g + (s & 1);
    u32 tag, arr;
    u32x4 P[6];
    POOL_INIT();
#define TAGS(v) ((v[1] ^ tag) | (v[3] ^ tag))
    {
      const bool qjob = s >= 16, mjob = s >= 4 && s < 16;
      const int job = qjob ? 16 * g + (s - 16) : 12 * g + (s - 4);
      const int pitch = qjob ? 8192 : 6144;
      const rsrc_t rc = mk(gc, 1024), rw = mk(qjob ? gwq : gwm, qjob ? ((ABL & 8) ? 0 : 512 * 8192) : (mjob && !(ABL & 4) ? 512 * 6144 : 0));
      u32x4 wv[8];
#pragma unroll
      for (int i = 0; i < 8; ++i) wv[i] = LD128(rw, (l >> 2) * 2 * pitch + (l & 3) * 16 + job * 64, (128 * w + (i >> 1) * 32 + (i & 1)) * pitch, AUX_NT);
      SB();
      u32 hp[4];
#pragma unroll
      for (int i = 0; i < 4; ++i) hp[i] = LD32(rc, (l >> 2) * 4 + i * 64, w * 256, 0);
      const u32 ep = LD32(rws, WS_EPOCH, 0, AUX_SC1);
      const u32x4 xv = LD128(rx, l * 16, 0, 0);
      const u32x2 xo = LD64(rx, (l & 31) * 8, w * 256, 0);
      SB();
      if (t < 8) *(volatile LAS u32*)(lds + L_FLAG + t * 4) = 0u;
      BAR();
      {
        const u32x4 sv = {SENT, SENT, SENT, SENT};
        const int pwo_ = WS_PW + ((2 * cs + (l >> 3)) * 32 + hh) * 128 + (l & 7) * 16;
        ST128(sv, rws, WS_PO + (((4 * g + w) * 8 + (l >> 3)) * 32 + s) * 128 + (l & 7) * 16, 0, AUX_SC1);
        const int o2 = w < 2 ? (ffn ? WS_PD + ((8 * w + (l >> 3)) * 224 + uj) * 128 + (l & 7) * 16 : OOB) : (w == 2 ? (l < 16 ? pwo_ : OOB) : (red && l < 8 ? WS_X1 + rid * 128 + l * 16 : OOB));
        ST128(sv, rws, o2, 0, AUX_SC1);
      }
      SB();
      tag = __builtin_amdgcn_readfirstlane(ep) + 1u;
      float xn[4];
      if (qjob) {
        float xs[8];
#pragma unroll
        for (int i = 0; i < 4; ++i) { xs[2 * i] = bfl(xv[i]); xs[2 * i + 1] = bfh(xv[i]); }
        const float c = __uint_as_float(__builtin_amdgcn_readfirstlane(__float_as_uint(xs[0])));
        float s1 = 0.f, s2 = 0.f;
#pragma unroll
        for (int i = 0; i < 8; ++i) { const float d = xs[i] - c; s1 += d; s2 = fmaf(d, d, s2); }
        const float ss = wave_sum2(s1, s2);
        float m, den;
        ln_fin(c, __uint_as_float(__builtin_amdgcn_readlane(__float_as_uint(ss), 0)), __uint_as_float(__builtin_amdgcn_readlane(__float_as_uint(ss), 32)), m, den);
        xn[0] = rbf(rbf(bfl(xo[0]) - m) * den); xn[1] = rbf(rbf(bfh(xo[0]) - m) * den);
        xn[2] = rbf(rbf(bfl(xo[1]) - m) * den); xn[3] = rbf(rbf(bfh(xo[1]) - m) * den);
#pragma unroll
        for (int i = 0; i < 4; ++i) PIN(xn[i]);
      } else {
#pragma unroll
        for (int i = 0; i < 4; ++i) { hp[i] = pk2(silu(bfl(hp[i])), silu(bfh(hp[i]))); PIN(hp[i]); }
      }
      u32 wp[32];
#pragma unroll
      for (int ip = 0; ip < 4; ++ip)
#pragma unroll
        for (int j = 0; j < 4; ++j) { wp[8 * ip + 2 * j] = PAIR_LO(wv[2 * ip + 1][j], wv[2 * ip][j]); wp[8 * ip + 2 * j + 1] = PAIR_HI(wv[2 * ip + 1][j], wv[2 * ip][j]); }
      if (qjob) {
#pragma unroll
        for (int i = 0; i < 32; ++i) PIN(wp[i]);
        const int vo = WS_MG + (l >> 5) * 2048 + (l & 31) * 16, so = w * 512;
        POLL(PH, 1, TAGS(P[0]) != 0u && !(ABL & 16), vo, so)
        STAMPW(2, 0);
        const auto e0 = __builtin_amdgcn_permlane32_swap(P[0][0], P[0][0], false, false), e1 = __builtin_amdgcn_permlane32_swap(P[0][2], P[0][2], false, false);
        const u32 h01 = pk2(rbf(xn[0] * rbf(1.f + bfl(e0[1]))) + bfl(e0[0]), rbf(xn[1] * rbf(1.f + bfh(e0[1]))) + bfh(e0[0]));
        const u32 h23 = pk2(rbf(xn[2] * rbf(1.f + bfl(e1[1]))) + bfl(e1[0]), rbf(xn[3] * rbf(1.f + bfh(e1[1]))) + bfh(e1[0]));
        *(LAS u32x2*)(lds + L_H + w * 256 + (l & 31) * 8) = u32x2{h01, h23};
#pragma unroll
        for (int i = 0; i < 4; ++i) hp[i] = *(LAS u32*)(lds + L_H + w * 256 + i * 64 + (l >> 2) * 4);
      }
      float r0, r1;
      gemv32(wp, hp, r0, r1);
      if (lc < 4) *(LAS f32x2*)(lds + L_FM + w * 128 + lc * 32 + lg * 8) = f32x2{r0, r1};
      if (!qjob) VM0();
      LGKM0();
      u32 old = 0;
      if (l == 0) old = lds_inc(lds + L_FLAG + F_CQ);
      arr = __builtin_amdgcn_readfirstlane(old);
      if (arr == 3u) {
        const int ro = L_FM + (l & 7) * 16;
        const f32x4 v = (*(LAS f32x4*)(lds + ro) + *(LAS f32x4*)(lds + ro + 128)) + (*(LAS f32x4*)(lds + ro + 256) + *(LAS f32x4*)(lds + ro + 384));
        ST128((u32x4{pk2(v[0], v[1]), tag, pk2(v[2], v[3]), tag}), rws, l < 8 && s >= 4 ? (qjob ? WS_QG : WS_MG) + job * 128 + l * 16 : OOB, 0, AUX_SC1);
        ST32(tag, rws, l == 0 ? WS_RDY + b * 4 : OOB, 0, AUX_SC1);
        if (qjob) STAMP(3); else if (mjob && job < 32) STAMP(1);
        if (!qjob) {
#pragma nounroll
          for (int i = 0; i < QG_DELAY; ++i) __builtin_amdgcn_s_sleep(16);
        }
        const int vo = WS_QG + g * 2048 + l * 16;
        POLL(PQ, 2, (TAGS(P[0]) | TAGS(P[1])) != 0u && !(ABL & 32), vo)
        *(LAS u32x2*)(lds + L_Q + l * 8) = u32x2{P[0][0], P[0][2]};
        *(LAS u32x2*)(lds + L_Q + 512 + l * 8) = u32x2{P[1][0], P[1][2]};
        LGKM0();
        *(volatile LAS u32*)(lds + L_FLAG + F_QRDY) = 1u;
        STAMP(4);
#pragma nounroll
        for (int sp = 0; sp < SPIN_CAP; ++sp) {
          MEMF();
          const u32x4 rd = LD128(rws, WS_RDY + l * 16, 0, AUX_SC1);
          if (((rd[0] ^ tag) | (rd[1] ^ tag) | (rd[2] ^ tag) | (rd[3] ^ tag)) == 0u) break;
          __builtin_amdgcn_s_sleep(4);
        }
        *(volatile LAS u32*)(lds + L_FLAG + F_RDY) = 1u;
        STAMP(19);
      }
    }
    u32 warm = 0;
    if (arr < 2u && !(ABL & 128)) {
      const rsrc_t rwx = mk(arr == 0u ? gwg : gwu, 512 * FF * 2);
      lds_wait_ge(lds + L_FLAG + F_WARM, 4u);
      warm = LD32(rwx, (16 * s + (l >> 2)) * (FF * 2) + (l & 3) * 128, (448 * g) & ~127, AUX_WARM);
    }
    SB();
    const int kA = 4 * l + w;
    float sbA, sbB, cbA, cbB, gbv;
    f32x4 ga4, x4;
    {
      const int mo = WS_MG + 6144 + (kA >> 1) * 8;
      u32x2 m2, m3, m4, m5, m6;
      u32x4 m0;
      const u32x2 xr = LD64(rx, rid * 64 + (l & 7) * 8, 0, 0);
      lds_wait_ge(lds + L_FLAG + F_QRDY, 1u);
#pragma nounroll
      for (int sp = 0; sp < SPIN_CAP; ++sp) {
        MEMF();
        m0 = LD128(rws, WS_MG + 4096 + rid * 128 + (l & 7) * 16, 0, AUX_SC1);
        m2 = LD64(rws, mo, 0, AUX_SC1); m3 = LD64(rws, mo + 1024, 0, AUX_SC1); m4 = LD64(rws, mo + 2048, 0, AUX_SC1); m5 = LD64(rws, mo + 3072, 0, AUX_SC1);
        m6 = LD64(rws, WS_MG + 10240 + ((fin & 31) >> 1) * 128 + (lc >> 2) * 32 + (fin & 1) * 16 + (lc & 2) * 4, 0, AUX_SC1);
        if ((TAGS(m0) | (m2[1] ^ tag) | (m3[1] ^ tag) | (m4[1] ^ tag) | (m5[1] ^ tag) | (m6[1] ^ tag)) == 0u) break;
        __builtin_amdgcn_s_sleep(4);
      }
      const bool hi = w & 1;
      sbA = hi ? bfh(m2[0]) : bfl(m2[0]); sbB = hi ? bfh(m3[0]) : bfl(m3[0]);
      cbA = rbf(1.f + (hi ? bfh(m4[0]) : bfl(m4[0]))); cbB = rbf(1.f + (hi ? bfh(m5[0]) : bfl(m5[0])));
      gbv = lc & 1 ? bfh(m6[0]) : bfl(m6[0]);
      ga4 = f32x4{bfl(m0[0]), bfh(m0[0]), bfl(m0[2]), bfh(m0[2])}; x4 = f32x4{bfl(xr[0]), bfh(xr[0]), bfl(xr[1]), bfh(xr[1])};
      PIN(sbA); PIN(sbB); PIN(cbA); PIN(cbB); PIN(gbv); PIN(ga4); PIN(x4);
    }
    const u32 hm = (lc & 1) ? 0xffff0000u : 0x0000ffffu;
    const int dw = (lc >> 1) & 3;
    u32 k0 = dw == 0 ? hm : 0u, k1 = dw == 1 ? hm : 0u, k2 = dw == 2 ? hm : 0u, k3 = dw == 3 ? hm : 0u;
    int p1m = l < 32 ? WS_PO + hh * 32768 + l * 128 + 64 : OOB, p1o = WS_PO + (hh * 8 + 2 * w) * 4096 + (l >> 2) * 128 + (l & 3) * 16;
    int r1w = (lc >> 3) == (lg >> 1) ? L_R1 + (2 * w + (lg >> 1)) * 256 + (lc & 7) * 32 + (lg & 1) * 16 : L_K + t * 16;
    int pwo = l < 16 ? WS_PW + ((2 * cs + (l >> 3)) * 32 + hh) * 128 + (l & 7) * 16 : OOB, p2o = WS_PW + rid * 4096 + l * 16;
    int x1o = l < 8 ? WS_X1 + rid * 128 + l * 16 : OOB, pxo = WS_X1 + l * 16;
    PIN(x1o); PIN(pxo);
    int r2w = (l & 6) == 0 ? L_R2 + ((l >> 3) * 2 + (l & 1)) * 16 + w * 4 : L_K + t * 16;
    int pdo = lg < 2 ? WS_PD + ((4 * w + (lc >> 2)) * 224 + uj) * 128 + lg * 64 + (lc & 3) * 16 : OOB;
    int p3o = WS_PD + ((fin & 31) >> 1) * (224 * 128) + (fin & 1) * 64 + (t >> 2) * 128 + (t & 3) * 16, p3x = t < 128 ? p3o + 3 * 8192 : OOB;
    int wdo = lg * 1024 + lc * 16;
    PIN(wdo); PIN(k0); PIN(k1); PIN(k2); PIN(k3); PIN(p1m); PIN(p1o); PIN(r1w); PIN(pwo); PIN(p2o); PIN(r2w); PIN(pdo); PIN(p3o); PIN(p3x);
#define DELTA(a) __builtin_bit_cast(bf16x8, u32x4{(a) & k0, (a) & k1, (a) & k2, (a) & k3})
    lds_wait_ge(lds + L_FLAG + F_ISS, 8u);
    u32x4 wov[4], wdv[2], wgA, wgB, wuA, wuB;
    {
      const rsrc_t rwo = mk(gwo, 4096 * 512 * 2);
#pragma unroll
      for (int n = 0; n < 4; ++n) wov[n] = LD128(rwo, (4 * (lc >> 3) + lg) * 1024 + (lc & 7) * 16, (hh * 128 + 32 * w + 8 * n) * 1024 + cs * 128, AUX_NT);
      SB();
      const u32 fb = ffn && !(ABL & 2) ? 512 * FF * 2 : 0;
      const rsrc_t rwg = mk(gwg, fb), rwu = mk(gwu, fb);
      wgA = LD128(rwg, kA * (FF * 2), uj * 16, AUX_NT); wgB = LD128(rwg, kA * (FF * 2), uj * 16 + 256 * FF * 2, AUX_NT);
      wuA = LD128(rwu, kA * (FF * 2), uj * 16, AUX_NT); wuB = LD128(rwu, kA * (FF * 2), uj * 16 + 256 * FF * 2, AUX_NT);
    }
    asm volatile("" :: "v"(warm));
    SB();
    STAMPW(22, 0);
    u32 gp[16];
#pragma unroll
    for (int j = 0; j < 4; ++j) {
      gp[2 * j] = PAIR_LO(wgB[j], wgA[j]); gp[2 * j + 1] = PAIR_HI(wgB[j], wgA[j]); gp[8 + 2 * j] = PAIR_LO(wuB[j], wuA[j]); gp[8 + 2 * j + 1] = PAIR_HI(wuB[j], wuA[j]);
    }
#pragma unroll
    for (int j = 0; j < 16; ++j) PIN(gp[j]);
    lds_wait_ge(lds + L_FLAG + F_RDY, 1u);
    lds_wait_ge(lds + L_FLAG + F_ATT, 8u);
    STAMPW(15, 0);
    {
      if (w == 0) { POLL(PM, 1, max(P[4][0], P[4][1]) == SENT, p1m) }
      STAMPW(17, 0);
      __builtin_amdgcn_s_barrier();
      P1_ISSUE(p1o);
      if (w == 0) {
        const float ms = l < 32 ? __uint_as_float(P[4][0]) : -INFINITY;
        float M = fmx(ms, dpp<XOR1>(ms));
        M = fmx(M, dpp<XOR2>(M)); M = fmx(M, dpp<ROR8>(M)); M = fmx(M, dpp<ROR4>(M)); M = sw16_max(M);
        const float e = __builtin_amdgcn_exp2f((ms - M) * L2E);
        float L = e * __uint_as_float(P[4][1]);
        L += dpp<XOR1>(L); L += dpp<XOR2>(L); L += dpp<ROR8>(L); L += dpp<ROR4>(L); L = sw16_add(L, L);
        if (l < 32) *(LAS float*)(lds + L_ST + l * 4) = e * rcp(L);
      }
      BAR();
      const float e0 = *(LAS float*)(lds + L_ST + (l >> 2) * 4), e1 = *(LAS float*)(lds + L_ST + 64 + (l >> 2) * 4);
#pragma nounroll
      for (int sp = 0; sp < SPIN_CAP; ++sp) {
        P1_WAIT(0);
        const bool pend = max(max(max4(P[0]), max4(P[1])), max(max4(P[2]), max4(P[3]))) == SENT;
        if (!__any(pend)) break;
        if (pend) P1_ISSUE(p1o);
      }
      STAMPW(9, 0);
      const f32x4 va = e0 * asf(P[0]) + e1 * asf(P[1]), vb = e0 * asf(P[2]) + e1 * asf(P[3]);
      float ya = sw16_add(sw32_add(va[0], va[2]), sw32_add(va[1], va[3])), yb = sw16_add(sw32_add(vb[0], vb[2]), sw32_add(vb[1], vb[3]));
      ya += dpp<ROR8>(ya); yb += dpp<ROR8>(yb); ya += dpp<ROR4>(ya); yb += dpp<ROR4>(yb);
      const u32 y2 = pk2(ya, yb);
      const u32 n0 = lc & 8 ? dppu<0x55>(y2) : dppu<0x00>(y2), n1 = lc & 8 ? dppu<0xFF>(y2) : dppu<0xAA>(y2);
      f32x4 d0 = {0.f, 0.f, 0.f, 0.f}, d1 = d0;
      d0 = __builtin_amdgcn_mfma_f32_16x16x32_bf16(DELTA(PAIR_LO(n0, n0)), __builtin_bit_cast(bf16x8, wov[0]), d0, 0, 0, 0);
      d1 = __builtin_amdgcn_mfma_f32_16x16x32_bf16(DELTA(PAIR_LO(n1, n1)), __builtin_bit_cast(bf16x8, wov[1]), d1, 0, 0, 0);
      d0 = __builtin_amdgcn_mfma_f32_16x16x32_bf16(DELTA(PAIR_HI(n0, n0)), __builtin_bit_cast(bf16x8, wov[2]), d0, 0, 0, 0);
      d1 = __builtin_amdgcn_mfma_f32_16x16x32_bf16(DELTA(PAIR_HI(n1, n1)), __builtin_bit_cast(bf16x8, wov[3]), d1, 0, 0, 0);
      *(LAS f32x4*)(lds + r1w) = d0 + d1;
      BAR();
      if (w == 0) {
        const f32x4 r = *(LAS f32x4*)(lds + L_R1 + (2 * lg) * 256 + lc * 16) + *(LAS f32x4*)(lds + L_R1 + (2 * lg + 1) * 256 + lc * 16);
        f32x4 q;
#pragma unroll
        for (int i = 0; i < 4; ++i) q[i] = sw16_add(r[i], r[i]);
#pragma unroll
        for (int i = 0; i < 4; ++i) q[i] = sw32_add(q[i], q[i]);
        ST128(canon4(q), rws, pwo, 0, AUX_SC1);
      }
      STAMPW(10, 0);
    }
    if (red && w == 1) {
      f32x4 v;
      P2_SUMS(v, p2o)
#pragma unroll
      for (int i = 0; i < 4; ++i) v[i] = sw32_add(v[i], v[i]);
#pragma unroll
      for (int i = 0; i < 4; ++i) v[i] = sw16_add(v[i], v[i]);
#pragma unroll
      for (int i = 0; i < 4; ++i) { v[i] += dpp<ROR8>(v[i]); v[i] = rbf(x4[i] + rbf(ga4[i] * rbf(v[i]))); }
      ST128(canon4(v), rws, x1o, 0, AUX_SC1);
      STAMP(18);
    }
    if (ffn) {
      {
        const rsrc_t rwd = mk(gwd, 512 * FF * 2);
        asm volatile("s_nop 4\n\tbuffer_load_dwordx4 %0, %2, %3, %4 offen nt\n\tbuffer_load_dwordx4 %1, %2, %3, %5 offen nt"
                     : "=&v"(wdv[0]), "=&v"(wdv[1]) : "v"(wdo), "s"(rwd), "s"(uj * 8192 + w * 256), "s"(uj * 8192 + 4096 + w * 256) : "memory");
      }
      if (w == 0) {
        POLL(PX, 2, max(max4(P[0]), max4(P[1])) == SENT, pxo)
        *(LAS u32x4*)(lds + L_XS + l * 16) = P[0]; *(LAS u32x4*)(lds + L_XS + 1024 + l * 16) = P[1];
      } else VM0();
      asm volatile("" : "+v"(wdv[0]), "+v"(wdv[1]));
      STAMPW(11, 0);
      BAR();
      const f32x4 xa = *(LAS f32x4*)(lds + L_XS + l * 16), xb = *(LAS f32x4*)(lds + L_XS + 1024 + l * 16);
      const float x1A = *(LAS float*)(lds + L_XS + kA * 4), x1B = *(LAS float*)(lds + L_XS + 1024 + kA * 4);
      const float cw = *(LAS float*)(lds + L_XS);
      const f32x4 da = xa - cw, db = xb - cw, d1 = da + db, d2 = da * da + db * db;
      const float ss = wave_sum2((d1[0] + d1[1]) + (d1[2] + d1[3]), (d2[0] + d2[1]) + (d2[2] + d2[3]));
      float m, den;
      ln_fin(cw, __uint_as_float(__builtin_amdgcn_readlane(__float_as_uint(ss), 0)), __uint_as_float(__builtin_amdgcn_readlane(__float_as_uint(ss), 32)), m, den);
      const u32 hp = pk2(rbf(rbf(rbf(x1A - m) * den) * cbA) + sbA, rbf(rbf(rbf(x1B - m) * den) * cbB) + sbB);
      float a[16];
#pragma unroll
      for (int j = 0; j < 16; ++j) a[j] = dot2(gp[j], hp, 0.f);
#pragma unroll
      for (int j = 0; j < 8; ++j) a[j] = sw32_add(a[j], a[j + 8]);
#pragma unroll
      for (int j = 0; j < 4; ++j) a[j] = sw16_add(a[j], a[j + 4]);
#pragma unroll
      for (int j = 0; j < 2; ++j) { const float lo = a[j] + dpp<ROR8>(a[j]), hi = a[j + 2] + dpp<ROR8>(a[j + 2]); a[j] = l & 8 ? hi : lo; }
      { const float lo = a[0] + dpp<XOR1>(a[0]), hi = a[1] + dpp<XOR1>(a[1]); a[0] = l & 1 ? hi : lo; }
      a[0] += dpp<XOR2>(a[0]);
      a[0] += dpp<0x141>(dpp<0x1B>(a[0]));
      *(LAS float*)(lds + r2w) = a[0];
      BAR();
      {
        float act[2];
#pragma unroll
        for (int n = 0; n < 2; ++n) {
          const int un = 4 * n + lg;
          const f32x4 gs = *(LAS f32x4*)(lds + L_R2 + un * 16), us = *(LAS f32x4*)(lds + L_R2 + (8 + un) * 16);
          const float gg = rbf((gs[0] + gs[1]) + (gs[2] + gs[3])), uu = rbf((us[0] + us[1]) + (us[2] + us[3]));
          const float z = gg * fmaf(gg * gg, -2.f * L2E * 0.7978845608028654f * 0.044715f, -2.f * L2E * 0.7978845608028654f);
          act[n] = rbf(gg * rcp(1.f + __builtin_amdgcn_exp2f(z))) * uu;
        }
        const u32 av = pk2(act[0], act[1]);
        f32x4 d = {0.f, 0.f, 0.f, 0.f};
        d = __builtin_amdgcn_mfma_f32_16x16x32_bf16(DELTA(PAIR_LO(av, av)), __builtin_bit_cast(bf16x8, wdv[0]), d, 0, 0, 0);
        d = __builtin_amdgcn_mfma_f32_16x16x32_bf16(DELTA(PAIR_HI(av, av)), __builtin_bit_cast(bf16x8, wdv[1]), d, 0, 0, 0);
        ST128(canon4(d), rws, pdo, 0, AUX_SC1);
      }
      STAMPW(12, 0);
      if (s < 4) {
        const int oc = 32 * (fin >> 1) + 8 * (lc >> 2) + 4 * (fin & 1) + (lc & 3);
        const float xo = *(LAS float*)(lds + L_XS + oc * 4);
        f32x4 v;
        P3_SUMS(v, p3o, p3x)
        STAMPW(13, 0);
        float y = sw16_add(sw32_add(v[0], v[2]), sw32_add(v[1], v[3]));
        y += dpp<ROR8>(y); y += dpp<ROR4>(y);
        if (lc < 4) *(LAS float*)(lds + L_R3 + (4 * lc + lg) * 16 + w * 4) = y;
        BAR();
        if (t < 16) {
          const f32x4 rs = *(LAS f32x4*)(lds + L_R3 + t * 16);
          gout[oc] = __builtin_bit_cast(u16, (__bf16)(xo + rbf(gbv * rbf((rs[0] + rs[1]) + (rs[2] + rs[3])))));
          if (b == 0 && t == 0) ST32(tag, rws, WS_EPOCH, 0, AUX_SC1);
        }
        STAMPW(14, 0);
      }
    }
    KEEP();
    while (__builtin_amdgcn_s_memrealtime() - tst_ < FIXT) __builtin_amdgcn_s_sleep(2);
  }
}

__global__ void __launch_bounds__(PT) probe_k(unsigned* p) {
  extern __shared__ unsigned char sm2_[];
  const u64 t_ = __builtin_amdgcn_s_memrealtime();
  while (__builtin_amdgcn_s_memrealtime() - t_ < 2000) __builtin_amdgcn_s_sleep(2);
  if (p == nullptr) sm2_[threadIdx.x] = 1;
}
extern "C" void kernel_launch(void* const* d_in, const int* in_sizes, int n_in, void* d_out, int out_size, void* d_ws, size_t ws_size, hipStream_t stream) {
  (void)hipFuncSetAttribute((const void*)probe_k, hipFuncAttributeMaxDynamicSharedMemorySize, 160000);
  probe_k<<<PG, PT, PL, stream>>>((unsigned*)d_ws);
  (void)hipFuncSetAttribute((const void*)fused_block, hipFuncAttributeMaxDynamicSharedMemorySize, L_END);
  fused_block<<<256, 768, L_END, stream>>>((const u16*)d_in[0], (const u16*)d_in[1], (const u16*)d_in[2], (const u16*)d_in[3], (const u16*)d_in[4],
                                           (const u16*)d_in[6], (const u16*)d_in[7], (const u16*)d_in[8], (const u16*)d_in[9], (const u16*)d_in[10],
                                           (u16*)d_out, (unsigned char*)d_ws);
}
```

```cpp
#include <hip/hip_runtime.h>
#include <hip/hip_bf16.h>

typedef unsigned short u16;
typedef unsigned int u32;
typedef unsigned long long u64;
typedef u32 u32x2 __attribute__((ext_vector_type(2)));
typedef u32 u32x4 __attribute__((ext_vector_type(4)));
typedef float f32x2 __attribute__((ext_vector_type(2)));
typedef float f32x4 __attribute__((ext_vector_type(4)));
typedef short s16x4 __attribute__((ext_vector_type(4)));
typedef __bf16 bf16x2 __attribute__((ext_vector_type(2)));
typedef __bf16 bf16x8 __attribute__((ext_vector_type(8)));
typedef __amdgpu_buffer_rsrc_t rsrc_t;
#define LAS __attribute__((address_space(3)))

constexpr int SCH0 = 25;
constexpr int SCHK = 33;
constexpr int SCHV = 30;
constexpr int T_MIN = 75;
constexpr int PH_T = 250;
constexpr int QG_T = 700;
constexpr int GW_T = 1133;
constexpr int H1_T = 1173;
constexpr int P2_T = 1318;
constexpr int WIN = 6;
constexpr int QPOS = 12;
constexpr int CODE_KB = 20;

constexpr int D = 512, SK = 16384, DH = 128, FF = 1792;
constexpr u32 SENT = 0xFFFFFFFFu;
constexpr int AUX_NT = 2, AUX_SC1 = 16;
constexpr int SPIN_CAP = 1 << 16;
constexpr int WS_EPOCH = 0;
constexpr int WS_RDY = 2048;
constexpr int WS_MG = 4096;
constexpr int WS_QG = WS_MG + 2 * 24576;
constexpr int WS_GA = WS_QG + 2 * 32768;
constexpr int WS_PW = WS_GA + 28 * 4 * 2048;
constexpr int WS_ML = WS_PW + 65536;
constexpr int WS_PO = WS_ML + 131072;
constexpr int WS_PD = WS_PO + 524288;
constexpr int WS_END = WS_PD + 16 * 56 * 128;
constexpr int OOB = 0x40000000;
constexpr int L_K = 0;
constexpr int L_ACC = 131072;
constexpr int L_Q = L_ACC + 16384;
constexpr int L_H = L_Q + 1024;
constexpr int L_M = L_H + 512;
constexpr int L_L = L_M + 128;
constexpr int L_R1 = L_L + 128;
constexpr int L_XS = L_R1 + 2048;
constexpr int L_ST = L_XS + 512;
constexpr int L_R2 = L_ST + 128;
constexpr int L_FM = L_R2 + 4096;
constexpr int L_FLAG = L_FM + 1024;
constexpr int F_CQ = 0, F_QRDY = 4, F_RDY = 8, F_WO = 12, F_PUB = 16, F_SM = 20, F_CP = 24, F_ORG = 28;
constexpr int L_END = L_FLAG + 64;
constexpr float L2E = 1.4426950408889634f;

__device__ __forceinline__ float bfl(u32 x) { return __uint_as_float(x << 16); }
__device__ __forceinline__ float bfh(u32 x) { return __uint_as_float(x & 0xffff0000u); }
__device__ __forceinline__ u32 pk2(float lo, float hi) { bf16x2 v = {(__bf16)lo, (__bf16)hi}; return __builtin_bit_cast(u32, v); }
__device__ __forceinline__ float rbf(float f) { return (float)(__bf16)f; }
__device__ __forceinline__ float fmx(float a, float b) { return __builtin_elementwise_maximum(a, b); }
template <int C> __device__ __forceinline__ u32 dppu(u32 x) { return (u32)__builtin_amdgcn_mov_dpp((int)x, C, 0xf, 0xf, true); }
template <int C> __device__ __forceinline__ float dpp(float x) { return __uint_as_float(dppu<C>(__float_as_uint(x))); }
constexpr int XOR1 = 0xB1, XOR2 = 0x4E, ROR4 = 0x124, ROR8 = 0x128;
__device__ __forceinline__ float sw16_add(float a, float b) {
  auto r = __builtin_amdgcn_permlane16_swap(__float_as_uint(a), __float_as_uint(b), false, false);
  return __uint_as_float(r[0]) + __uint_as_float(r[1]);
}
__device__ __forceinline__ float sw32_add(float a, float b) {
  auto r = __builtin_amdgcn_permlane32_swap(__float_as_uint(a), __float_as_uint(b), false, false);
  return __uint_as_float(r[0]) + __uint_as_float(r[1]);
}
__device__ __forceinline__ float sw16_max(float a) {
  auto r = __builtin_amdgcn_permlane16_swap(__float_as_uint(a), __float_as_uint(a), false, false);
  return fmx(__uint_as_float(r[0]), __uint_as_float(r[1]));
}
__device__ __forceinline__ float sw32_max(float a) {
  auto r = __builtin_amdgcn_permlane32_swap(__float_as_uint(a), __float_as_uint(a), false, false);
  return fmx(__uint_as_float(r[0]), __uint_as_float(r[1]));
}
__device__ __forceinline__ float wave_sum2(float a, float b) {
  float x = sw32_add(a, b);
  x = sw16_add(x, x);
  x += dpp<XOR1>(x); x += dpp<XOR2>(x); x += dpp<ROR8>(x); x += dpp<ROR4>(x);
  return x;
}
__device__ __forceinline__ rsrc_t mk(const void* p, u32 bytes) { return __builtin_amdgcn_make_buffer_rsrc((void*)p, 0, bytes, 0x00020000); }
#define LD128(r, v, s, a) __builtin_amdgcn_raw_buffer_load_b128(r, v, s, a)
#define LD64(r, v, s, a) __builtin_amdgcn_raw_buffer_load_b64(r, v, s, a)
#define LD32(r, v, s, a) __builtin_amdgcn_raw_buffer_load_b32(r, v, s, a)
#define ST128(d, r, v, s, a) __builtin_amdgcn_raw_buffer_store_b128(d, r, v, s, a)
#define ST32(d, r, v, s, a) __builtin_amdgcn_raw_buffer_store_b32(d, r, v, s, a)
__device__ __forceinline__ u32 canon(float f) { return min(__float_as_uint(f), 0xFFFFFFFEu); }
__device__ __forceinline__ u32x4 canon4(f32x4 v) { return u32x4{canon(v[0]), canon(v[1]), canon(v[2]), canon(v[3])}; }
__device__ __forceinline__ u32 max4(u32x4 v) { return max(max(v[0], v[1]), max(v[2], v[3])); }
__device__ __forceinline__ f32x4 asf(u32x4 v) { return __builtin_bit_cast(f32x4, v); }
__device__ __forceinline__ float rcp(float x) { return __builtin_amdgcn_rcpf(x); }
__device__ __forceinline__ float silu(float x) { return x / (1.f + expf(-x)); }
__device__ __forceinline__ float dot2(u32 a, u32 b, float c) { return __builtin_amdgcn_fdot2_f32_bf16(__builtin_bit_cast(bf16x2, a), __builtin_bit_cast(bf16x2, b), c, false); }
#define MEMF() asm volatile("" ::: "memory")
#define LGKM0() asm volatile("s_waitcnt lgkmcnt(0)" ::: "memory")
#define VM0() asm volatile("s_waitcnt vmcnt(0)" ::: "memory")
#define BAR() do { LGKM0(); __builtin_amdgcn_s_barrier(); MEMF(); } while (0)
#define SB() __builtin_amdgcn_sched_barrier(0)
#define PACE() asm volatile("s_waitcnt vmcnt(%0)" :: "i"(WIN - 1) : "memory")
#define PIN(x) asm volatile("" : "+v"(x))
#define NOW() ((u32)__builtin_amdgcn_s_memrealtime())
#define UNTIL(tt) do { while ((int)(NOW() - tstart) < (tt)) __builtin_amdgcn_s_sleep(1); MEMF(); } while (0)
__device__ __forceinline__ u32 lds_inc(LAS unsigned char* p) { return __hip_atomic_fetch_add((LAS u32*)p, 1u, __ATOMIC_RELAXED, __HIP_MEMORY_SCOPE_WORKGROUP); }
__device__ __forceinline__ void lds_wait_ge(LAS unsigned char* p, u32 n) {
#pragma nounroll
  for (int sp = 0; sp < SPIN_CAP * 16; ++sp) { if (*(volatile LAS u32*)p >= n) break; __builtin_amdgcn_s_sleep(1); }
  MEMF();
}
__device__ __forceinline__ void ln_fin(float c, float S1, float S2, float& m, float& den) {
  const float d = S1 * (1.f / D);
  const float var = rbf(fmaxf(fmaf(-d, d, S2 * (1.f / D)), 0.f));
  m = rbf(c + d); den = rbf(__builtin_amdgcn_sqrtf(rbf(var + 1e-5f)));
}
__device__ __forceinline__ void gemv64(const u32 (&wp)[32], const u32 (&hp)[4], float& r0, float& r1) {
  float a[8];
#pragma unroll
  for (int j = 0; j < 8; ++j) a[j] = dot2(wp[24 + j], hp[3], dot2(wp[16 + j], hp[2], dot2(wp[8 + j], hp[1], dot2(wp[j], hp[0], 0.f))));
#pragma unroll
  for (int j = 0; j < 4; ++j) a[j] = sw32_add(a[j], a[j + 4]);
#pragma unroll
  for (int j = 0; j < 2; ++j) a[j] = sw16_add(a[j], a[j + 2]);
  r0 = a[0] + dpp<ROR8>(a[0]); r1 = a[1] + dpp<ROR8>(a[1]);
}
#define PAIR_LO(hi, lo) __builtin_amdgcn_perm(hi, lo, 0x05040100u)
#define PAIR_HI(hi, lo) __builtin_amdgcn_perm(hi, lo, 0x07060302u)
#define PH_ISSUE0(vo_, so_) asm volatile( \
      "s_nop 4\n\t" \
      "buffer_load_dwordx4 v[100:103], %[vo], %[rs], %[so] offen sc1\n\t" \
      "buffer_load_dwordx4 v[104:107], %[vo], %[rs], %[s1] offen sc1\n\t" \
      : "=&{v[100:103]}"(P[0]), "=&{v[104:107]}"(P[1]) : [vo] "v"(vo_), [so] "s"(so_), [s1] "s"((so_) + 24576), [rs] "s"(rws) : "memory")
#define PH_ISSUE(vo_, so_) asm volatile( \
      "s_nop 4\n\t" \
      "buffer_load_dwordx4 v[100:103], %[vo], %[rs], %[so] offen sc1\n\t" \
      "buffer_load_dwordx4 v[104:107], %[vo], %[rs], %[s1] offen sc1\n\t" \
      : "+{v[100:103]}"(P[0]), "+{v[104:107]}"(P[1]) : [vo] "v"(vo_), [so] "s"(so_), [s1] "s"((so_) + 24576), [rs] "s"(rws) : "memory")
#define PQ_TAGS(vo_) { int cnt_ = SPIN_CAP, np_; u64 st_; asm volatile( \
      "s_nop 4\n\t" \
      "buffer_load_dwordx4 v[100:103], %[vo], %[rs], 0 offen sc1\n\t" \
      "buffer_load_dwordx4 v[104:107], %[vo], %[rs], 0 offen offset:1024 sc1\n\t" \
      "buffer_load_dwordx4 v[108:111], %[vo], %[rs], 0 offen offset:2048 sc1\n\t" \
      "buffer_load_dwordx4 v[112:115], %[vo], %[rs], 0 offen offset:3072 sc1\n\t" \
      "buffer_load_dwordx4 v[116:119], %[vo], %[rs], %[s1] offen sc1\n\t" \
      "buffer_load_dwordx4 v[120:123], %[vo], %[rs], %[s1] offen offset:1024 sc1\n\t" \
      "buffer_load_dwordx4 v[124:127], %[vo], %[rs], %[s1] offen offset:2048 sc1\n\t" \
      "buffer_load_dwordx4 v[128:131], %[vo], %[rs], %[s1] offen offset:3072 sc1\n\t" \
      "1:\n\t" \
      "s_waitcnt vmcnt(0)\n\t" \
      "s_mov_b32 %[np], 0\n\t" \
      "v_cmp_ne_u32 vcc, %[tag], v101\n\t" \
      "v_cmp_ne_u32 %[st], %[tag], v103\n\t" \
      "s_or_b64 exec, vcc, %[st]\n\t" \
      "s_cbranch_execz 5f\n\t" \
      "buffer_load_dwordx4 v[100:103], %[vo], %[rs], 0 offen sc1\n\t" \
      "s_mov_b32 %[np], 1\n\t" \
      "5:\n\t" \
      "s_mov_b64 exec, -1\n\t" \
      "v_cmp_ne_u32 vcc, %[tag], v105\n\t" \
      "v_cmp_ne_u32 %[st], %[tag], v107\n\t" \
      "s_or_b64 exec, vcc, %[st]\n\t" \
      "s_cbranch_execz 5f\n\t" \
      "buffer_load_dwordx4 v[104:107], %[vo], %[rs], 0 offen offset:1024 sc1\n\t" \
      "s_mov_b32 %[np], 1\n\t" \
      "5:\n\t" \
      "s_mov_b64 exec, -1\n\t" \
      "v_cmp_ne_u32 vcc, %[tag], v109\n\t" \
      "v_cmp_ne_u32 %[st], %[tag], v111\n\t" \
      "s_or_b64 exec, vcc, %[st]\n\t" \
      "s_cbranch_execz 5f\n\t" \
      "buffer_load_dwordx4 v[108:111], %[vo], %[rs], 0 offen offset:2048 sc1\n\t" \
      "s_mov_b32 %[np], 1\n\t" \
      "5:\n\t" \
      "s_mov_b64 exec, -1\n\t" \
      "v_cmp_ne_u32 vcc, %[tag], v113\n\t" \
      "v_cmp_ne_u32 %[st], %[tag], v115\n\t" \
      "s_or_b64 exec, vcc, %[st]\n\t" \
      "s_cbranch_execz 5f\n\t" \
      "buffer_load_dwordx4 v[112:115], %[vo], %[rs], 0 offen offset:3072 sc1\n\t" \
      "s_mov_b32 %[np], 1\n\t" \
      "5:\n\t" \
      "s_mov_b64 exec, -1\n\t" \
      "v_cmp_ne_u32 vcc, %[tag], v117\n\t" \
      "v_cmp_ne_u32 %[st], %[tag], v119\n\t" \
      "s_or_b64 exec, vcc, %[st]\n\t" \
      "s_cbranch_execz 5f\n\t" \
      "buffer_load_dwordx4 v[116:119], %[vo], %[rs], %[s1] offen sc1\n\t" \
      "s_mov_b32 %[np], 1\n\t" \
      "5:\n\t" \
      "s_mov_b64 exec, -1\n\t" \
      "v_cmp_ne_u32 vcc, %[tag], v121\n\t" \
      "v_cmp_ne_u32 %[st], %[tag], v123\n\t" \
      "s_or_b64 exec, vcc, %[st]\n\t" \
      "s_cbranch_execz 5f\n\t" \
      "buffer_load_dwordx4 v[120:123], %[vo], %[rs], %[s1] offen offset:1024 sc1\n\t" \
      "s_mov_b32 %[np], 1\n\t" \
      "5:\n\t" \
      "s_mov_b64 exec, -1\n\t" \
      "v_cmp_ne_u32 vcc, %[tag], v125\n\t" \
      "v_cmp_ne_u32 %[st], %[tag], v127\n\t" \
      "s_or_b64 exec, vcc, %[st]\n\t" \
      "s_cbranch_execz 5f\n\t" \
      "buffer_load_dwordx4 v[124:127], %[vo], %[rs], %[s1] offen offset:2048 sc1\n\t" \
      "s_mov_b32 %[np], 1\n\t" \
      "5:\n\t" \
      "s_mov_b64 exec, -1\n\t" \
      "v_cmp_ne_u32 vcc, %[tag], v129\n\t" \
      "v_cmp_ne_u32 %[st], %[tag], v131\n\t" \
      "s_or_b64 exec, vcc, %[st]\n\t" \
      "s_cbranch_execz 5f\n\t" \
      "buffer_load_dwordx4 v[128:131], %[vo], %[rs], %[s1] offen offset:3072 sc1\n\t" \
      "s_mov_b32 %[np], 1\n\t" \
      "5:\n\t" \
      "s_mov_b64 exec, -1\n\t" \
      "s_cmp_eq_u32 %[np], 0\n\t" \
      "s_cbranch_scc1 2f\n\t" \
      "s_sub_u32 %[cnt], %[cnt], 1\n\t" \
      "s_cbranch_scc0 1b\n\t" \
      "s_waitcnt vmcnt(0)\n\t" \
      "2:\n\t" \
      "s_nop 0\n\t" \
      : "=&{v[100:103]}"(P[0]), "=&{v[104:107]}"(P[1]), "=&{v[108:111]}"(P[2]), "=&{v[112:115]}"(P[3]), "=&{v[116:119]}"(P[4]), "=&{v[120:123]}"(P[5]), "=&{v[124:127]}"(P[6]), "=&{v[128:131]}"(P[7]), [cnt] "+&s"(cnt_), [np] "=&s"(np_), [st] "=&s"(st_) \
      : [vo] "v"(vo_), [s1] "s"(32768), [rs] "s"(rws), [tag] "s"(tag) : "vcc", "scc", "memory"); }
#define H1_GATHER(vo_, vm_) { int cnt_ = SPIN_CAP, np_; f32x4 t0_; asm volatile( \
      "s_nop 4\n\t" \
      "4:\n\t" \
      "buffer_load_dwordx2 v[116:117], %[vm], %[rs], 0 offen sc1\n\t" \
      "buffer_load_dwordx2 v[118:119], %[vm], %[rs], 0 offen offset:1024 sc1\n\t" \
      "buffer_load_dwordx2 v[120:121], %[vm], %[rs], 0 offen offset:2048 sc1\n\t" \
      "buffer_load_dwordx2 v[122:123], %[vm], %[rs], 0 offen offset:3072 sc1\n\t" \
      "buffer_load_dwordx4 v[100:103], %[vo], %[rs], 0 offen sc1\n\t" \
      "buffer_load_dwordx4 v[104:107], %[vo], %[rs], 0 offen offset:1024 sc1\n\t" \
      "buffer_load_dwordx4 v[108:111], %[vo], %[rs], 0 offen offset:2048 sc1\n\t" \
      "buffer_load_dwordx4 v[112:115], %[vo], %[rs], 0 offen offset:3072 sc1\n\t" \
      "1:\n\t" \
      "s_waitcnt vmcnt(0)\n\t" \
      "v_max3_u32 v84, v100, v101, v102\n\t" \
      "v_max3_u32 v85, v103, v104, v105\n\t" \
      "v_max3_u32 v86, v106, v107, v108\n\t" \
      "v_max3_u32 v87, v109, v110, v111\n\t" \
      "v_max3_u32 v84, v84, v112, v113\n\t" \
      "v_max3_u32 v85, v85, v114, v115\n\t" \
      "v_max3_u32 v86, v86, v116, v117\n\t" \
      "v_max3_u32 v87, v87, v118, v119\n\t" \
      "v_max3_u32 v84, v84, v120, v121\n\t" \
      "v_max3_u32 v85, v85, v122, v123\n\t" \
      "v_max3_u32 v84, v84, v86, v87\n\t" \
      "v_max_u32 v84, v84, v85\n\t" \
      "v_cmp_eq_u32 vcc, -1, v84\n\t" \
      "s_cbranch_vccz 2f\n\t" \
      "s_sub_u32 %[cnt], %[cnt], 1\n\t" \
      "s_cbranch_scc1 2f\n\t" \
      "s_mov_b32 %[np], 0\n\t" \
      "v_cmp_eq_u32 vcc, -1, v116\n\t" \
      "s_mov_b64 exec, vcc\n\t" \
      "s_cbranch_execz 5f\n\t" \
      "buffer_load_dwordx2 v[116:117], %[vm], %[rs], 0 offen sc1\n\t" \
      "s_mov_b32 %[np], 1\n\t" \
      "5:\n\t" \
      "s_mov_b64 exec, -1\n\t" \
      "v_cmp_eq_u32 vcc, -1, v118\n\t" \
      "s_mov_b64 exec, vcc\n\t" \
      "s_cbranch_execz 5f\n\t" \
      "buffer_load_dwordx2 v[118:119], %[vm], %[rs], 0 offen offset:1024 sc1\n\t" \
      "s_mov_b32 %[np], 1\n\t" \
      "5:\n\t" \
      "s_mov_b64 exec, -1\n\t" \
      "v_cmp_eq_u32 vcc, -1, v120\n\t" \
      "s_mov_b64 exec, vcc\n\t" \
      "s_cbranch_execz 5f\n\t" \
      "buffer_load_dwordx2 v[120:121], %[vm], %[rs], 0 offen offset:2048 sc1\n\t" \
      "s_mov_b32 %[np], 1\n\t" \
      "5:\n\t" \
      "s_mov_b64 exec, -1\n\t" \
      "v_cmp_eq_u32 vcc, -1, v122\n\t" \
      "s_mov_b64 exec, vcc\n\t" \
      "s_cbranch_execz 5f\n\t" \
      "buffer_load_dwordx2 v[122:123], %[vm], %[rs], 0 offen offset:3072 sc1\n\t" \
      "s_mov_b32 %[np], 1\n\t" \
      "5:\n\t" \
      "s_mov_b64 exec, -1\n\t" \
      "v_cmp_eq_u32 vcc, -1, v100\n\t" \
      "s_mov_b64 exec, vcc\n\t" \
      "s_cbranch_execz 5f\n\t" \
      "buffer_load_dwordx4 v[100:103], %[vo], %[rs], 0 offen sc1\n\t" \
      "s_mov_b32 %[np], 1\n\t" \
      "5:\n\t" \
      "s_mov_b64 exec, -1\n\t" \
      "v_cmp_eq_u32 vcc, -1, v104\n\t" \
      "s_mov_b64 exec, vcc\n\t" \
      "s_cbranch_execz 5f\n\t" \
      "buffer_load_dwordx4 v[104:107], %[vo], %[rs], 0 offen offset:1024 sc1\n\t" \
      "s_mov_b32 %[np], 1\n\t" \
      "5:\n\t" \
      "s_mov_b64 exec, -1\n\t" \
      "v_cmp_eq_u32 vcc, -1, v108\n\t" \
      "s_mov_b64 exec, vcc\n\t" \
      "s_cbranch_execz 5f\n\t" \
      "buffer_load_dwordx4 v[108:111], %[vo], %[rs], 0 offen offset:2048 sc1\n\t" \
      "s_mov_b32 %[np], 1\n\t" \
      "5:\n\t" \
      "s_mov_b64 exec, -1\n\t" \
      "v_cmp_eq_u32 vcc, -1, v112\n\t" \
      "s_mov_b64 exec, vcc\n\t" \
      "s_cbranch_execz 5f\n\t" \
      "buffer_load_dwordx4 v[112:115], %[vo], %[rs], 0 offen offset:3072 sc1\n\t" \
      "s_mov_b32 %[np], 1\n\t" \
      "5:\n\t" \
      "s_mov_b64 exec, -1\n\t" \
      "s_cmp_eq_u32 %[np], 0\n\t" \
      "s_cbranch_scc1 4b\n\t" \
      "s_branch 1b\n\t" \
      "2:\n\t" \
      "s_nop 0\n\t" \
      : "=&{v[100:103]}"(P[0]), "=&{v[104:107]}"(P[1]), "=&{v[108:111]}"(P[2]), "=&{v[112:115]}"(P[3]), "=&{v[116:119]}"(P[4]), "=&{v[120:123]}"(P[5]), "=&{v[84:87]}"(t0_), [cnt] "+&s"(cnt_), [np] "=&s"(np_) \
      : [vo] "v"(vo_), [vm] "v"(vm_), [rs] "s"(rws) : "vcc", "scc", "memory"); }
#define P2_SUMS(r0_, vo_) { int cnt_ = SPIN_CAP / 4, np_; f32x4 t0_; u32x4 P[4]; asm volatile( \
      "s_nop 4\n\t" \
      "4:\n\t" \
      "buffer_load_dwordx4 v[100:103], %[vo], %[rs], 0 offen sc1\n\t" \
      "buffer_load_dwordx4 v[104:107], %[vo], %[rs], 0 offen offset:1024 sc1\n\t" \
      "buffer_load_dwordx4 v[108:111], %[vo], %[rs], 0 offen offset:2048 sc1\n\t" \
      "buffer_load_dwordx4 v[112:115], %[vo], %[rs], 0 offen offset:3072 sc1\n\t" \
      "1:\n\t" \
      "s_waitcnt vmcnt(0)\n\t" \
      "v_pk_add_f32 v[92:93], v[100:101], v[104:105]\n\t" \
      "v_pk_add_f32 v[94:95], v[102:103], v[106:107]\n\t" \
      "v_pk_add_f32 v[84:85], v[108:109], v[112:113]\n\t" \
      "v_pk_add_f32 v[86:87], v[110:111], v[114:115]\n\t" \
      "v_pk_add_f32 v[92:93], v[92:93], v[84:85]\n\t" \
      "v_pk_add_f32 v[94:95], v[94:95], v[86:87]\n\t" \
      "s_nop 0\n\t" \
      "v_pk_add_f32 v[84:85], v[92:93], v[94:95]\n\t" \
      "s_nop 0\n\t" \
      "v_cmp_u_f32 vcc, v84, v85\n\t" \
      "s_cbranch_vccz 2f\n\t" \
      "s_sub_u32 %[cnt], %[cnt], 1\n\t" \
      "s_cbranch_scc1 2f\n\t" \
      "s_mov_b32 %[np], 0\n\t" \
      "v_cmp_eq_u32 vcc, -1, v100\n\t" \
      "s_mov_b64 exec, vcc\n\t" \
      "s_cbranch_execz 5f\n\t" \
      "buffer_load_dwordx4 v[100:103], %[vo], %[rs], 0 offen sc1\n\t" \
      "s_mov_b32 %[np], 1\n\t" \
      "5:\n\t" \
      "s_mov_b64 exec, -1\n\t" \
      "v_cmp_eq_u32 vcc, -1, v104\n\t" \
      "s_mov_b64 exec, vcc\n\t" \
      "s_cbranch_execz 5f\n\t" \
      "buffer_load_dwordx4 v[104:107], %[vo], %[rs], 0 offen offset:1024 sc1\n\t" \
      "s_mov_b32 %[np], 1\n\t" \
      "5:\n\t" \
      "s_mov_b64 exec, -1\n\t" \
      "v_cmp_eq_u32 vcc, -1, v108\n\t" \
      "s_mov_b64 exec, vcc\n\t" \
      "s_cbranch_execz 5f\n\t" \
      "buffer_load_dwordx4 v[108:111], %[vo], %[rs], 0 offen offset:2048 sc1\n\t" \
      "s_mov_b32 %[np], 1\n\t" \
      "5:\n\t" \
      "s_mov_b64 exec, -1\n\t" \
      "v_cmp_eq_u32 vcc, -1, v112\n\t" \
      "s_mov_b64 exec, vcc\n\t" \
      "s_cbranch_execz 5f\n\t" \
      "buffer_load_dwordx4 v[112:115], %[vo], %[rs], 0 offen offset:3072 sc1\n\t" \
      "s_mov_b32 %[np], 1\n\t" \
      "5:\n\t" \
      "s_mov_b64 exec, -1\n\t" \
      "s_cmp_eq_u32 %[np], 0\n\t" \
      "s_cbranch_scc1 4b\n\t" \
      "s_branch 1b\n\t" \
      "2:\n\t" \
      "s_nop 0\n\t" \
      : "=&{v[100:103]}"(P[0]), "=&{v[104:107]}"(P[1]), "=&{v[108:111]}"(P[2]), "=&{v[112:115]}"(P[3]), "=&{v[92:95]}"(r0_), "=&{v[84:87]}"(t0_), [cnt] "+&s"(cnt_), [np] "=&s"(np_) \
      : [vo] "v"(vo_), [rs] "s"(rws) : "vcc", "scc", "memory"); }
#define PA_ISSUE0(vo_, vs_) asm volatile( \
      "s_nop 4\n\t" \
      "buffer_load_dwordx4 v[100:103], %[vo], %[rs], 0 offen sc1\n\t" \
      "buffer_load_dwordx4 v[104:107], %[vs], %[rs], 0 offen sc1\n\t" \
      "buffer_load_dwordx4 v[108:111], %[vo], %[rs], 0 offen offset:512 sc1\n\t" \
      "buffer_load_dwordx4 v[112:115], %[vo], %[rs], 0 offen offset:1024 sc1\n\t" \
      : "=&{v[100:103]}"(P[0]), "=&{v[104:107]}"(P[1]), "=&{v[108:111]}"(P[2]), "=&{v[112:115]}"(P[3]) : [vo] "v"(vo_), [vs] "v"(vs_), [rs] "s"(rws) : "memory")
#define PA_ISSUE(vo_, vs_) asm volatile( \
      "s_nop 4\n\t" \
      "buffer_load_dwordx4 v[100:103], %[vo], %[rs], 0 offen sc1\n\t" \
      "buffer_load_dwordx4 v[104:107], %[vs], %[rs], 0 offen sc1\n\t" \
      "buffer_load_dwordx4 v[108:111], %[vo], %[rs], 0 offen offset:512 sc1\n\t" \
      "buffer_load_dwordx4 v[112:115], %[vo], %[rs], 0 offen offset:1024 sc1\n\t" \
      : "+{v[100:103]}"(P[0]), "+{v[104:107]}"(P[1]), "+{v[108:111]}"(P[2]), "+{v[112:115]}"(P[3]) : [vo] "v"(vo_), [vs] "v"(vs_), [rs] "s"(rws) : "memory")
#define PH_WAIT0() asm volatile("s_waitcnt vmcnt(0)" : "+{v[100:103]}"(P[0]), "+{v[104:107]}"(P[1]) :: "memory")
#define PH_WAITN() asm volatile("s_waitcnt vmcnt(2)" : "+{v[100:103]}"(P[0]), "+{v[104:107]}"(P[1]) :: "memory")
#define PH_TIE() asm volatile("" : "+{v[100:103]}"(P[0]), "+{v[104:107]}"(P[1]))
#define PH_KEEP() asm volatile("; keep" :: "{v[100:103]}"(P[0]), "{v[104:107]}"(P[1]))
#define PA_WAIT0() asm volatile("s_waitcnt vmcnt(0)" : "+{v[100:103]}"(P[0]), "+{v[104:107]}"(P[1]), "+{v[108:111]}"(P[2]), "+{v[112:115]}"(P[3]) :: "memory")
#define PA_WAITN() asm volatile("s_waitcnt vmcnt(4)" : "+{v[100:103]}"(P[0]), "+{v[104:107]}"(P[1]), "+{v[108:111]}"(P[2]), "+{v[112:115]}"(P[3]) :: "memory")
#define PA_TIE() asm volatile("" : "+{v[100:103]}"(P[0]), "+{v[104:107]}"(P[1]), "+{v[108:111]}"(P[2]), "+{v[112:115]}"(P[3]))
#define PA_KEEP() asm volatile("; keep" :: "{v[100:103]}"(P[0]), "{v[104:107]}"(P[1]), "{v[108:111]}"(P[2]), "{v[112:115]}"(P[3]))
#define PF_SUMS(r0_, vo_, v3_) { int cnt_ = SPIN_CAP / 4, np_; f32x4 t0_; u32x4 P[4]; asm volatile( \
      "s_nop 4\n\t" \
      "4:\n\t" \
      "buffer_load_dwordx4 v[100:103], %[vo], %[rs], 0 offen sc1\n\t" \
      "buffer_load_dwordx4 v[104:107], %[vo], %[rs], 0 offen offset:2048 sc1\n\t" \
      "buffer_load_dwordx4 v[108:111], %[vo], %[rs], %[s1] offen sc1\n\t" \
      "buffer_load_dwordx4 v[112:115], %[v3], %[rs], %[s1] offen offset:2048 sc1\n\t" \
      "1:\n\t" \
      "s_waitcnt vmcnt(0)\n\t" \
      "v_pk_add_f32 v[92:93], v[100:101], v[104:105]\n\t" \
      "v_pk_add_f32 v[94:95], v[102:103], v[106:107]\n\t" \
      "v_pk_add_f32 v[84:85], v[108:109], v[112:113]\n\t" \
      "v_pk_add_f32 v[86:87], v[110:111], v[114:115]\n\t" \
      "v_pk_add_f32 v[92:93], v[92:93], v[84:85]\n\t" \
      "v_pk_add_f32 v[94:95], v[94:95], v[86:87]\n\t" \
      "s_nop 0\n\t" \
      "v_pk_add_f32 v[84:85], v[92:93], v[94:95]\n\t" \
      "s_nop 0\n\t" \
      "v_cmp_u_f32 vcc, v84, v85\n\t" \
      "s_cbranch_vccz 2f\n\t" \
      "s_sub_u32 %[cnt], %[cnt], 1\n\t" \
      "s_cbranch_scc1 2f\n\t" \
      "s_mov_b32 %[np], 0\n\t" \
      "v_cmp_eq_u32 vcc, -1, v100\n\t" \
      "s_mov_b64 exec, vcc\n\t" \
      "s_cbranch_execz 5f\n\t" \
      "buffer_load_dwordx4 v[100:103], %[vo], %[rs], 0 offen sc1\n\t" \
      "s_mov_b32 %[np], 1\n\t" \
      "5:\n\t" \
      "s_mov_b64 exec, -1\n\t" \
      "v_cmp_eq_u32 vcc, -1, v104\n\t" \
      "s_mov_b64 exec, vcc\n\t" \
      "s_cbranch_execz 5f\n\t" \
      "buffer_load_dwordx4 v[104:107], %[vo], %[rs], 0 offen offset:2048 sc1\n\t" \
      "s_mov_b32 %[np], 1\n\t" \
      "5:\n\t" \
      "s_mov_b64 exec, -1\n\t" \
      "v_cmp_eq_u32 vcc, -1, v108\n\t" \
      "s_mov_b64 exec, vcc\n\t" \
      "s_cbranch_execz 5f\n\t" \
      "buffer_load_dwordx4 v[108:111], %[vo], %[rs], %[s1] offen sc1\n\t" \
      "s_mov_b32 %[np], 1\n\t" \
      "5:\n\t" \
      "s_mov_b64 exec, -1\n\t" \
      "v_cmp_eq_u32 vcc, -1, v112\n\t" \
      "s_mov_b64 exec, vcc\n\t" \
      "s_cbranch_execz 5f\n\t" \
      "buffer_load_dwordx4 v[112:115], %[v3], %[rs], %[s1] offen offset:2048 sc1\n\t" \
      "s_mov_b32 %[np], 1\n\t" \
      "5:\n\t" \
      "s_mov_b64 exec, -1\n\t" \
      "s_cmp_eq_u32 %[np], 0\n\t" \
      "s_cbranch_scc1 4b\n\t" \
      "s_branch 1b\n\t" \
      "2:\n\t" \
      "s_nop 0\n\t" \
      : "=&{v[100:103]}"(P[0]), "=&{v[104:107]}"(P[1]), "=&{v[108:111]}"(P[2]), "=&{v[112:115]}"(P[3]), "=&{v[92:95]}"(r0_), "=&{v[84:87]}"(t0_), [cnt] "+&s"(cnt_), [np] "=&s"(np_) \
      : [vo] "v"(vo_), [v3] "v"(v3_), [s1] "s"(4096), [rs] "s"(rws) : "vcc", "scc", "memory"); }

#define POLL(NAME, PEND, ...)                                                                     \
  NAME##_ISSUE0(__VA_ARGS__); NAME##_WAIT0();                                                    \
  { bool pend_ = PEND;                                                                            \
    if (__any(pend_)) {                                                                           \
      if (pend_) NAME##_ISSUE(__VA_ARGS__);                                                       \
      __builtin_amdgcn_s_sleep(4);                                                                \
      _Pragma("nounroll") for (int sp = 0; sp < SPIN_CAP; ++sp) {                                 \
        if (pend_) NAME##_ISSUE(__VA_ARGS__);                                                     \
        NAME##_WAITN(); pend_ = PEND;                                                            \
        if (!__any(pend_)) break;                                                                 \
      } }                                                                                         \
    NAME##_TIE(); }

__global__ void __launch_bounds__(768, 3)
fused_block(const u16* __restrict__ gx, const u16* __restrict__ gk, const u16* __restrict__ gv, const u16* __restrict__ gwq,
            const u16* __restrict__ gwo, const u16* __restrict__ gwg, const u16* __restrict__ gwu, const u16* __restrict__ gwd,
            const u16* __restrict__ gc, const u16* __restrict__ gwm, u16* __restrict__ gout, unsigned char* __restrict__ gws) {
  extern __shared__ __attribute__((aligned(16))) unsigned char smem[];
  LAS unsigned char* lds = (LAS unsigned char*)smem;
  const int b = blockIdx.x, g = b & 7, s = b >> 3;
  const int t = threadIdx.x, w = __builtin_amdgcn_readfirstlane(t >> 6), l = t & 63, lg = l >> 4, lc = l & 15;
  const rsrc_t rws = mk(gws, WS_END);
  const u32 tentry = NOW();
#define ORIGIN() (tentry + max((int)(NOW() - tentry), T_MIN))
  const int lb = (int)(size_t)lds;
  if (w >= 9) {
    u64 pc;
    asm volatile("s_getpc_b64 %0" : "=s"(pc));
    const rsrc_t rcode = mk((const void*)(pc & ~127ull), CODE_KB * 1024);
    asm volatile("s_mov_b32 m0, %2\n\ts_nop 4\n\tbuffer_load_dword %0, %1, 0 offen lds" :: "v"(((w - 9) * 64 + l) * 128), "s"(rcode), "s"(lb + L_ACC + w * 256) : "memory");
  }
  asm volatile("" :: "s"(gx), "s"(gk), "s"(gv), "s"(gwq), "s"(gwo), "s"(gwg), "s"(gwu), "s"(gwd), "s"(gc), "s"(gwm), "s"(gout), "s"(gws));
  if (__builtin_expect(w >= 4, 0)) {
    const int a = w - 4;
    const size_t kvo = ((size_t)g * SK + s * 512) * DH;
    const rsrc_t rk = mk(gk + kvo, 131072), rv = mk(gv + kvo, 131072);
    __builtin_amdgcn_s_barrier();
    const u32 tstart = ORIGIN();
#define SCHED(tt) do { PACE(); UNTIL(tt); } while (0)
#pragma nounroll
    for (int i = 0; i < 16; ++i) {
      const int so = a * 16384 + i * 1024, vk = lg * 256 + ((lc ^ (4 * lg + (i & 3))) * 16);
      SCHED(SCH0 + i * SCHK);
      asm volatile("s_mov_b32 m0, %3\n\ts_nop 4\n\tbuffer_load_dwordx4 %0, %1, %2 offen nt lds" :: "v"(vk), "s"(rk), "s"(so), "s"(lb + L_K + so) : "memory");
    }
    u32x4 vf[16];
#define VLOAD(i) do { SB(); SCHED(SCH0 + 16 * SCHK + (i) * SCHV); vf[i] = LD128(rv, l * 16 + ((i) & 3) * 1024, a * 16384 + ((i) >> 2) * 4096, AUX_NT); SB(); } while (0)
#pragma unroll
    for (int i = 0; i < QPOS; ++i) VLOAD(i);
#define VNEXT(k) do { if (QPOS + (k) < 16) VLOAD((QPOS + (k)) & 15); } while (0)
    const u32 hm = (lc & 1) ? 0xffff0000u : 0x0000ffffu;
    u32 m0 = (lc & 2) ? 0u : hm, m1 = (lc & 2) ? hm : 0u;
    int ka[4], pubo = l < 32 ? WS_PO + (((4 * g + (a & 3)) * 4 + (l >> 3)) * 32 + s) * 128 + (l & 7) * 16 : (l < 40 ? WS_ML + ((4 * g + (a & 3)) * 32 + s) * 128 + (l & 7) * 16 : OOB);
#pragma unroll
    for (int tt = 0; tt < 4; ++tt) { ka[tt] = L_K + a * 16384 + (4 * (lc & 3) + (lc >> 2)) * 256 + (((4 * tt + lg) ^ lc) * 16); PIN(ka[tt]); }
    PIN(m0); PIN(m1); PIN(pubo);
    lds_wait_ge(lds + L_FLAG + F_QRDY, 1u);
    u32x4 qf[4];
#pragma unroll
    for (int tt = 0; tt < 4; ++tt) qf[tt] = *(LAS u32x4*)(lds + L_Q + (lc >> 2) * 256 + tt * 64 + lg * 16);
    asm volatile("s_waitcnt vmcnt(%0)" :: "i"(QPOS) : "memory");
    f32x4 sc;
    {
      f32x4 c[4];
#pragma unroll
      for (int T = 0; T < 4; ++T) {
        c[T] = f32x4{0.f, 0.f, 0.f, 0.f};
#pragma unroll
        for (int tt = 0; tt < 4; ++tt)
          c[T] = __builtin_amdgcn_mfma_f32_16x16x32_bf16(__builtin_bit_cast(bf16x8, *(LAS u32x4*)(lds + ka[tt] + T * 4096)), __builtin_bit_cast(bf16x8, qf[tt]), c[T], 0, 0, 0);
        if (T & 1) VNEXT(T >> 1);
      }
      const f32x4 lo = (l & 1) ? c[1] : c[0], hi = (l & 1) ? c[3] : c[2];
      sc = (l & 2) ? hi : lo;
    }
#pragma unroll
    for (int r = 0; r < 4; ++r) sc[r] = rbf(rbf(sc[r]) * 0.08838834764831845f);
    float mw = fmx(fmx(sc[0], sc[1]), fmx(sc[2], sc[3]));
    mw = fmx(mw, dpp<XOR1>(mw)); mw = fmx(mw, dpp<XOR2>(mw)); mw = sw32_max(sw16_max(mw));
    u32 pp[4];
    float lw = 0.f;
#pragma unroll
    for (int r = 0; r < 4; ++r) { const float p = __builtin_amdgcn_exp2f((sc[r] - mw) * L2E); lw += p; pp[r] = pk2(p, p); }
    VNEXT(2);
    f32x4 o0 = {0.f, 0.f, 0.f, 0.f}, o1 = {0.f, 0.f, 0.f, 0.f};
#define PV(i, Q) { const u32 x_ = dppu<Q>(pp[(i) & 3]); const u32x2 pa_ = {x_ & m0, x_ & m1};                                                         \
      o0 = __builtin_amdgcn_mfma_f32_16x16x16bf16_1k(__builtin_bit_cast(s16x4, pa_), __builtin_bit_cast(s16x4, u32x2{vf[i][0], vf[i][1]}), o0, 0, 0, 0); \
      o1 = __builtin_amdgcn_mfma_f32_16x16x16bf16_1k(__builtin_bit_cast(s16x4, pa_), __builtin_bit_cast(s16x4, u32x2{vf[i][2], vf[i][3]}), o1, 0, 0, 0); }
    lw += dpp<XOR1>(lw); lw += dpp<XOR2>(lw); lw = sw16_add(lw, lw); lw = sw32_add(lw, lw);
    if (lg == 0 && (lc & 3) == 0) { *(LAS float*)(lds + L_M + (lc >> 2) * 32 + a * 4) = mw; *(LAS float*)(lds + L_L + (lc >> 2) * 32 + a * 4) = lw; }
    LGKM0();
    if (l == 0) lds_inc(lds + L_FLAG + F_SM);
    PV(0, 0x00) PV(1, 0x00) PV(2, 0x00) PV(3, 0x00)
    VNEXT(3);
    PV(4, 0x55) PV(5, 0x55) PV(6, 0x55) PV(7, 0x55)
    VNEXT(4);
    VNEXT(5);
    PV(8, 0xAA) PV(9, 0xAA)
    VNEXT(6);
    PV(10, 0xAA)
    VNEXT(7);
    static_assert(QPOS >= 8 && QPOS >= WIN && QPOS <= 16, "every V load goes out ahead of its use");
    float e[8], M = 0.f, L = 0.f;
    if (a < 4) {
      lds_wait_ge(lds + L_FLAG + F_SM, 8u);
      const f32x4 ma = *(LAS f32x4*)(lds + L_M + a * 32), mb = *(LAS f32x4*)(lds + L_M + a * 32 + 16);
      const f32x4 la = *(LAS f32x4*)(lds + L_L + a * 32), lb = *(LAS f32x4*)(lds + L_L + a * 32 + 16);
      M = fmx(fmx(fmx(ma[0], ma[1]), fmx(ma[2], ma[3])), fmx(fmx(mb[0], mb[1]), fmx(mb[2], mb[3])));
#pragma unroll
      for (int i = 0; i < 8; ++i) {
        e[i] = __builtin_amdgcn_exp2f(((i < 4 ? ma[i & 3] : mb[i & 3]) - M) * L2E);
        L = fmaf(e[i], i < 4 ? la[i & 3] : lb[i & 3], L);
        PIN(e[i]);
      }
      PIN(L);
    }
    SB();
    PV(11, 0xAA) PV(12, 0xFF)
    SB();
    if (l == 0) lds_inc(lds + L_FLAG + F_WO);
    SB();
    PV(13, 0xFF) PV(14, 0xFF) PV(15, 0xFF)
    __builtin_amdgcn_s_setprio(3);
    *(LAS f32x4*)(lds + L_ACC + a * 2048 + lg * 512 + lc * 32) = o0;
    *(LAS f32x4*)(lds + L_ACC + a * 2048 + lg * 512 + lc * 32 + 16) = o1;
    BAR();
    if (a < 4) {
      const int ao = L_ACC + a * 512 + (l & 31) * 16;
      f32x4 oa = e[0] * *(LAS f32x4*)(lds + ao), ob = e[1] * *(LAS f32x4*)(lds + ao + 2048);
#pragma unroll
      for (int i = 2; i < 8; i += 2) { oa += e[i] * *(LAS f32x4*)(lds + ao + i * 2048); ob += e[i + 1] * *(LAS f32x4*)(lds + ao + (i + 1) * 2048); }
      f32x4 o = oa + ob;
      if (l >= 32) o = f32x4{M, L, 0.f, 0.f};
      ST128(canon4(o), rws, pubo, 0, AUX_SC1);
      MEMF();
      if (l == 0) lds_inc(lds + L_FLAG + F_PUB);
    }
  } else {
    const rsrc_t rx = mk(gx, 1024);
    const int hh = 4 * g + (((s - 8) >> 2) & 3), cs = s & 3, uj = 7 * g + s, fin = 4 * g + s;
    const bool wot = s >= 8 && s < 24, gut = s < 8 || (s >= 24 && s < 30), ffn = s < 7;
    const int kb = g >> 1, cb = 14 * (g & 1) + (s < 8 ? s : s - 16), gao = WS_GA + (cb * 4 + kb) * 2048;
    u32 tag, arr;
    u32 tstart;
#define TAGS(v) ((v[1] ^ tag) | (v[3] ^ tag))
#define RDY_POLL(n) do { _Pragma("nounroll") for (int sp = 0; sp < (gut || wot ? (n) : 0); ++sp) { MEMF();                                            \
        const u32x4 ra = LD128(rws, WS_RDY + l * 16, 0, AUX_SC1), rb = LD128(rws, WS_RDY + 1024 + l * 16, 0, AUX_SC1);                                \
        if (__all((TAGS(ra) | TAGS(rb)) == 0u) || sp == SPIN_CAP - 1) {                                                                               \
          int dl = max(max((int)(ra[0] - tstart), (int)(ra[2] - tstart)), max((int)(rb[0] - tstart), (int)(rb[2] - tstart)));                         \
          _Pragma("unroll") for (int o = 32; o > 0; o >>= 1) dl = max(dl, __shfl_xor(dl, o));                                                         \
          *(volatile LAS u32*)(lds + L_FLAG + F_ORG) = tstart + min(dl, 200);                       \
          LGKM0(); *(volatile LAS u32*)(lds + L_FLAG + F_RDY) = 1u; break; }                                                                          \
      } } while (0)
#define RDY_WAIT() do { if (arr == 3u && *(volatile LAS u32*)(lds + L_FLAG + F_RDY) == 0u) RDY_POLL(SPIN_CAP); lds_wait_ge(lds + L_FLAG + F_RDY, 1u);  \
        tstart = __builtin_amdgcn_readfirstlane(*(volatile LAS u32*)(lds + L_FLAG + F_ORG)); } while (0)
    {
      const bool qjob = s >= 16, mjob = s >= 4 && s < 16;
      const int hf = s & 1, line = qjob ? 8 * g + ((s - 16) >> 1) : g + 8 * ((s - 4) >> 1);
      const int k0 = 256 * hf + 64 * w;
      u32 old = 0;
      u32x4 P[8];
      P[0] = P[1] = u32x4{0u, 0u, 0u, 0u};
#define POISON() do { const u32x4 sv = {SENT, SENT, SENT, SENT};                                                                                       \
        ST128(sv, rws, l < 32 ? WS_PO + (((4 * g + w) * 4 + (l >> 3)) * 32 + s) * 128 + (l & 7) * 16 : (l < 40 ? WS_ML + ((4 * g + w) * 32 + s) * 128 + (l & 7) * 16 : OOB), 0, AUX_SC1); \
        const int o2 = w < 2 ? (ffn ? WS_PD + ((8 * w + (l >> 3)) * 56 + uj) * 128 + (l & 7) * 16 : (w == 0 && wot && l < 32 ? WS_PW + ((4 * cs + (l >> 3)) * 32 + hh) * 128 + (l & 7) * 16 : OOB)) \
                             : (gut ? gao + (w - 2) * 1024 + l * 16 : OOB);                                                                            \
        ST128(sv, rws, o2, 0, AUX_SC1); } while (0)
#define POISONED() do { u32 oc_ = 0; if (l == 0) oc_ = lds_inc(lds + L_FLAG + F_CP); __builtin_amdgcn_raw_buffer_store_b64(u32x2{tstart, tag}, rws, l == 0 && oc_ == 3u ? WS_RDY + b * 8 : OOB, 0, AUX_SC1); } while (0)
      const u32 ep = LD32(rws, WS_EPOCH, 0, AUX_SC1);
      if (qjob || mjob) {
        const int pitch = qjob ? 8192 : 6144;
        const rsrc_t rc = mk(gc, 1024), rw = mk(qjob ? gwq : gwm, 512 * pitch);
        u32 hp[4];
#pragma unroll
        for (int i = 0; i < 4; ++i) hp[i] = LD32(rc, (l >> 3) * 4 + i * 32, k0 * 2, 0);
        const u32x4 xv = LD128(rx, l * 16, 0, 0);
        const u32 xo = LD32(rx, (l & 31) * 4, k0 * 2, 0);
        u32x4 wv[8];
#pragma unroll
        for (int i = 0; i < 8; ++i) wv[i] = LD128(rw, (l >> 3) * 2 * pitch + (l & 7) * 16 + line * 128, (k0 + (i >> 1) * 16 + (i & 1)) * pitch, AUX_NT);
        SB();
        if (t < 8) *(volatile LAS u32*)(lds + L_FLAG + t * 4) = 0u;
        BAR();
        tstart = ORIGIN();
        tag = __builtin_amdgcn_readfirstlane(ep) + 1u;
        float xn0, xn1;
        if (qjob) {
          float xs[8];
#pragma unroll
          for (int i = 0; i < 4; ++i) { xs[2 * i] = bfl(xv[i]); xs[2 * i + 1] = bfh(xv[i]); }
          const float c = __uint_as_float(__builtin_amdgcn_readfirstlane(__float_as_uint(xs[0])));
          float s1 = 0.f, s2 = 0.f;
#pragma unroll
          for (int i = 0; i < 8; ++i) { const float d = xs[i] - c; s1 += d; s2 = fmaf(d, d, s2); }
          const float ss = wave_sum2(s1, s2);
          float m, den;
          ln_fin(c, __uint_as_float(__builtin_amdgcn_readlane(__float_as_uint(ss), 0)), __uint_as_float(__builtin_amdgcn_readlane(__float_as_uint(ss), 32)), m, den);
          xn0 = rbf(rbf(bfl(xo) - m) / den); xn1 = rbf(rbf(bfh(xo) - m) / den);
          PIN(xn0); PIN(xn1);
        } else {
#pragma unroll
          for (int i = 0; i < 4; ++i) { hp[i] = pk2(silu(bfl(hp[i])), silu(bfh(hp[i]))); PIN(hp[i]); }
        }
        u32 wp[32];
#pragma unroll
        for (int ip = 0; ip < 4; ++ip)
#pragma unroll
          for (int j = 0; j < 4; ++j) { wp[8 * ip + 2 * j] = PAIR_LO(wv[2 * ip + 1][j], wv[2 * ip][j]); wp[8 * ip + 2 * j + 1] = PAIR_HI(wv[2 * ip + 1][j], wv[2 * ip][j]); }
        if (qjob) {
#pragma unroll
          for (int i = 0; i < 32; ++i) PIN(wp[i]);
          POISON();
          const int vo = WS_MG + (l >> 5) * 4096 + (l & 31) * 16, so = k0 * 8;
          UNTIL(PH_T);
          POLL(PH, (TAGS(P[0]) | TAGS(P[1])) != 0u, vo, so)
          const u32 mp = pk2(__uint_as_float(P[0][0]) + __uint_as_float(P[1][0]), __uint_as_float(P[0][2]) + __uint_as_float(P[1][2]));
          const auto e0 = __builtin_amdgcn_permlane32_swap(mp, mp, false, false);
          *(LAS u32*)(lds + L_H + w * 128 + (l & 31) * 4) = pk2(rbf(xn0 * rbf(1.f + bfl(e0[1]))) + bfl(e0[0]), rbf(xn1 * rbf(1.f + bfh(e0[1]))) + bfh(e0[0]));
#pragma unroll
          for (int i = 0; i < 4; ++i) hp[i] = *(LAS u32*)(lds + L_H + w * 128 + i * 32 + (l >> 3) * 4);
        }
        float r0, r1;
        gemv64(wp, hp, r0, r1);
        if (lc < 8) *(LAS f32x2*)(lds + L_FM + w * 256 + lc * 32 + lg * 8) = f32x2{r0, r1};
        LGKM0();
      } else {
        if (t < 8) *(volatile LAS u32*)(lds + L_FLAG + t * 4) = 0u;
        BAR();
        tstart = ORIGIN();
        tag = __builtin_amdgcn_readfirstlane(ep) + 1u;
      }
      if (l == 0) old = lds_inc(lds + L_FLAG + F_CQ);
      arr = __builtin_amdgcn_readfirstlane(old);
      if (arr == 3u && (qjob || mjob)) {
        const int ro = L_FM + (l & 31) * 8;
        const f32x2 v = (*(LAS f32x2*)(lds + ro) + *(LAS f32x2*)(lds + ro + 256)) + (*(LAS f32x2*)(lds + ro + 512) + *(LAS f32x2*)(lds + ro + 768));
        ST128((u32x4{__float_as_uint(v[0]), tag, __float_as_uint(v[1]), tag}), rws, l < 32 ? (qjob ? WS_QG + hf * 32768 : WS_MG + hf * 24576) + line * 512 + l * 16 : OOB, 0, AUX_SC1);
      }
      if (!qjob) POISON();
      VM0();
      PH_KEEP();
      POISONED();
      if (arr == 3u) {
        UNTIL(QG_T);
        const int vo = WS_QG + g * 4096 + l * 16;
        PQ_TAGS(vo)
#pragma unroll
        for (int i = 0; i < 4; ++i)
          *(LAS u32*)(lds + L_Q + i * 256 + l * 4) = pk2(__uint_as_float(P[i][0]) + __uint_as_float(P[4 + i][0]), __uint_as_float(P[i][2]) + __uint_as_float(P[4 + i][2]));
        lds_wait_ge(lds + L_FLAG + F_CP, 4u);
        LGKM0();
        *(volatile LAS u32*)(lds + L_FLAG + F_QRDY) = 1u;
        RDY_POLL(1);
      }
    }
    const u32 hm = (lc & 1) ? 0xffff0000u : 0x0000ffffu;
    const int dw = (lc >> 1) & 3;
    u32 k0 = dw == 0 ? hm : 0u, k1 = dw == 1 ? hm : 0u, k2 = dw == 2 ? hm : 0u, k3 = dw == 3 ? hm : 0u;
    int p1m = WS_ML + (hh * 32 + (l >> 3)) * 128, p1o = WS_PO + (hh * 4 + w) * 4096 + l * 16;
    int r1w = lg < 2 ? L_R1 + w * 512 + lc * 32 + lg * 16 : L_K + t * 16;
    int pwo = l < 32 ? WS_PW + ((4 * cs + (l >> 3)) * 32 + hh) * 128 + (l & 7) * 16 : OOB;
    int r1r = L_R1 + (l & 31) * 16;
    PIN(k0); PIN(k1); PIN(k2); PIN(k3); PIN(p1m); PIN(p1o); PIN(r1w); PIN(pwo); PIN(r1r);
#define WSUM(o) ((*(LAS f32x4*)(lds + (o)) + *(LAS f32x4*)(lds + (o) + 512)) + (*(LAS f32x4*)(lds + (o) + 1024) + *(LAS f32x4*)(lds + (o) + 1536)))
#define DELTA(a) __builtin_bit_cast(bf16x8, u32x4{(a) & k0, (a) & k1, (a) & k2, (a) & k3})
#define MV(wv, n, d) d = __builtin_amdgcn_mfma_f32_16x16x32_bf16(DELTA(dppu<0x150 + (n)>(av)), __builtin_bit_cast(bf16x8, wv[n]), d, 0, 0, 0);
#define MV8(wv) SB(); MV(wv, 0, d0) MV(wv, 1, d1) MV(wv, 2, d0) MV(wv, 3, d1) MV(wv, 4, d0) MV(wv, 5, d1) MV(wv, 6, d0) MV(wv, 7, d1)         \
    __builtin_amdgcn_sched_group_barrier(0x2, 8, 0);                                                                                       \
    _Pragma("unroll") for (int i_ = 0; i_ < 6; ++i_) { __builtin_amdgcn_sched_group_barrier(0x8, 1, 0); __builtin_amdgcn_sched_group_barrier(0x2, 4, 0); } \
    __builtin_amdgcn_sched_group_barrier(0x8, 2, 0); SB();
    if (wot) {
      u32x4 wov[8], P[6];
      const rsrc_t rwo = mk(gwo, 4096 * 512 * 2);
      const int woo = lg * 1024 + lc * 16;
#define WO_LOAD(n) asm volatile("s_nop 4\n\tbuffer_load_dwordx4 %0, %1, %2, %3 offen nt" : "=&v"(wov[n]) : "v"(woo), "s"(rwo), "s"((hh * 128 + 32 * w + 4 * (n)) * 1024 + cs * 256) : "memory")
      lds_wait_ge(lds + L_FLAG + F_WO, 8u);
#pragma unroll
      for (int n = 0; n < 8; ++n) WO_LOAD(n);
      __builtin_amdgcn_s_barrier();
      RDY_WAIT();
      UNTIL(H1_T);
      H1_GATHER(p1o, p1m)
#pragma unroll
      for (int n = 0; n < 8; ++n) PIN(wov[n]);
      const f32x4 ms = {__uint_as_float(P[4][0]), __uint_as_float(P[4][2]), __uint_as_float(P[5][0]), __uint_as_float(P[5][2])};
      float M = fmx(fmx(ms[0], ms[1]), fmx(ms[2], ms[3]));
      M = fmx(M, dpp<ROR8>(M)); M = sw32_max(sw16_max(M));
      f32x4 e4;
#pragma unroll
      for (int i = 0; i < 4; ++i) e4[i] = __builtin_amdgcn_exp2f((ms[i] - M) * L2E);
      float L = fmaf(e4[0], __uint_as_float(P[4][1]), e4[1] * __uint_as_float(P[4][3])) + fmaf(e4[2], __uint_as_float(P[5][1]), e4[3] * __uint_as_float(P[5][3]));
      const f32x4 v = (e4[0] * asf(P[0]) + e4[1] * asf(P[1])) + (e4[2] * asf(P[2]) + e4[3] * asf(P[3]));
      L = sw32_add(L, L); L = sw16_add(L, L); L += dpp<ROR8>(L);
      float y = sw16_add(sw32_add(v[0], v[2]), sw32_add(v[1], v[3]));
      y += dpp<ROR8>(y);
      y *= rcp(L);
      const u32 av = pk2(y, y);
      f32x4 d0 = {0.f, 0.f, 0.f, 0.f}, d1 = d0;
      MV8(wov)
      *(LAS f32x4*)(lds + r1w) = d0 + d1;
      BAR();
      if (w == 0) {
        const f32x4 q = WSUM(r1r);
        ST128(canon4(q), rws, pwo, 0, AUX_SC1);
      }
    }
    if (gut) {
      const int col = 128 * kb + 32 * w + 4 * (l & 7) + lg;
      const int oc = 32 * ((fin & 31) >> 1) + 8 * (l & 3) + 4 * (fin & 1) + lg;
      float gav, sbv, cbv, xv, gbv;
      {
        const int mo = WS_MG + (1024 + col) * 8, bo = WS_MG + (2560 + oc) * 8;
        u32x2 m[2][4];
        const u32 xr = __builtin_amdgcn_raw_buffer_load_b16(rx, col * 2, 0, 0);
        lds_wait_ge(lds + L_FLAG + F_QRDY, 1u);
#pragma nounroll
        for (int sp = 0; sp < SPIN_CAP; ++sp) {
          MEMF();
          u32 bad = 0;
#pragma unroll
          for (int h2 = 0; h2 < 2; ++h2) {
#pragma unroll
            for (int i = 0; i < 3; ++i) m[h2][i] = LD64(rws, mo, h2 * 24576 + i * 4096, AUX_SC1);
            m[h2][3] = LD64(rws, bo, h2 * 24576, AUX_SC1);
          }
#pragma unroll
          for (int i = 0; i < 8; ++i) bad |= m[i >> 2][i & 3][1] ^ tag;
          if (bad == 0u) break;
          __builtin_amdgcn_s_sleep(4);
        }
#define MSUM(i) rbf(__uint_as_float(m[0][i][0]) + __uint_as_float(m[1][i][0]))
        gav = MSUM(0); sbv = MSUM(1); cbv = rbf(1.f + MSUM(2)); gbv = MSUM(3); xv = bfl(xr);
        PIN(gav); PIN(sbv); PIN(cbv); PIN(gbv); PIN(xv);
      }
      auto row_pairs = [&](float v, u32& p0, u32& p1) {
        const u32 e = pk2(v, dpp<XOR1>(v));
        p0 = (u32)__builtin_amdgcn_update_dpp((int)dppu<0x150>(e), (int)e, 0x154, 0xf, 0xc, false);
        p1 = (u32)__builtin_amdgcn_update_dpp((int)dppu<0x152>(e), (int)e, 0x156, 0xf, 0xc, false);
      };
      u32 gp[32];
      int r2w = (l & 8) == 0 ? L_R2 + w * 512 + ((l >> 2) & 1) * 256 + (l >> 5) * 128 + (l & 3) * 32 + (lg & 1) * 16 : L_K + t * 16;
      int r2r = L_R2 + (l & 31) * 16;
      auto tile_dot = [&](u32 p0, u32 p1, float (&a)[16]) -> f32x4 {
#pragma unroll
        for (int j = 0; j < 16; ++j) a[j] = dot2(gp[(j >> 3) * 16 + 8 + (j & 7)], p1, dot2(gp[(j >> 3) * 16 + (j & 7)], p0, a[j]));
#pragma unroll
        for (int j = 0; j < 8; ++j) a[j] = sw32_add(a[j], a[j + 8]);
#pragma unroll
        for (int j = 0; j < 4; ++j) a[j] = sw16_add(a[j], a[j + 4]);
        f32x4 r;
#pragma unroll
        for (int j = 0; j < 4; ++j) r[j] = a[j] + dpp<ROR8>(a[j]);
        return r;
      };
      f32x4 bw;
      u32x4 wdv[8];
      const rsrc_t rwd = mk(gwd, ffn ? 512 * FF * 2 : 0);
      {
        const rsrc_t rwg = mk(gwg, 512 * FF * 2), rwu = mk(gwu, 512 * FF * 2);
        const int guo = (16 * ((l >> 3) & 1) + lg) * (FF * 2) + (l & 7) * 16;
        u32 b0, b1, c0, c1;
        row_pairs(cbv, b0, b1); row_pairs(sbv, c0, c1);
        PIN(b0); PIN(b1); PIN(c0); PIN(c1); PIN(r2w); PIN(r2r);
        __builtin_amdgcn_s_barrier();
        lds_wait_ge(lds + L_FLAG + F_PUB, 4u);
        RDY_WAIT();
        UNTIL(GW_T);
        u32x4 gv_[8];
#pragma unroll
        for (int i = 0; i < 8; ++i) gv_[i] = LD128(i < 4 ? rwg : rwu, guo, cb * 128 + (128 * kb + 32 * w + 4 * (i & 3)) * (FF * 2), AUX_NT);
#pragma unroll
        for (int n = 0; n < 8; ++n) wdv[n] = LD128(rwd, lg * 1024 + lc * 16, uj * 32768 + n * 4096 + w * 256, AUX_NT);
        SB();
#pragma unroll
        for (int ip = 0; ip < 4; ++ip)
#pragma unroll
          for (int j = 0; j < 4; ++j) { gp[8 * ip + 2 * j] = PAIR_LO(gv_[2 * ip + 1][j], gv_[2 * ip][j]); gp[8 * ip + 2 * j + 1] = PAIR_HI(gv_[2 * ip + 1][j], gv_[2 * ip][j]); }
        float ab[16] = {}, ac[16] = {};
        bw = tile_dot(b0, b1, ab);
        *(LAS f32x4*)(lds + r2w) = bw;
        *(LAS f32x4*)(lds + r2w + 2048) = tile_dot(c0, c1, ac);
        BAR();
        if (w == 1) ST128(canon4(WSUM(r2r + (l >> 5) * 2048)), rws, gao + 512 + l * 16, 0, AUX_SC1);
        BAR();
      }
      int p2o = WS_PW + (4 * kb + w) * 4096 + l * 16, stw = (l & 31) == 0 ? L_ST + w * 16 + 4 + (l >> 5) * 4 : (l == 1 ? L_ST + w * 16 : L_K + t * 16);
      int gaw = w == 0 ? (l < 32 ? gao + l * 16 : OOB) : (l < 8 ? gao + 1536 + l * 16 : OOB);
      int pao = WS_GA + ((uj >> 1) * 4 + lg) * 2048 + (uj & 1) * 256 + lc * 16;
      int pas = WS_GA + ((uj >> 1) * 4 + (lc >> 2)) * 2048 + 1536 + (l & 3) * 16;
      int pdo = lg < 2 ? WS_PD + ((4 * w + (lc >> 2)) * 56 + uj) * 128 + lg * 64 + (lc & 3) * 16 : OOB;
      int xsw = s < 4 ? L_XS + (col & 127) * 4 : L_K + t * 16 + 4;
      float aa[16];
#pragma unroll
      for (int j = 0; j < 16; ++j) { aa[j] = 0.f; PIN(aa[j]); }
#pragma unroll
      for (int n = 0; n < 8; ++n) PIN(wdv[n]);
      PIN(p2o); PIN(stw); PIN(gaw); PIN(pao); PIN(pas); PIN(pdo); PIN(xsw);
#define RED4(v) sw16_add(sw32_add(v[0], v[2]), sw32_add(v[1], v[3]))
      f32x4 v;
      UNTIL(P2_T);
      P2_SUMS(v, p2o)
      float y = RED4(v);
      y += dpp<ROR8>(y);
      const float x1 = rbf(xv + rbf(gav * rbf(y)));
      const float cw = __uint_as_float(__builtin_amdgcn_readfirstlane(__float_as_uint(x1)));
      const float dx = x1 - cw;
      const float ss = 0.5f * wave_sum2(dx, dx * dx);
      u32 y0, y1;
      row_pairs(dx * cbv, y0, y1);
      *(LAS f32x4*)(lds + r2w) = tile_dot(y0, y1, aa) + cw * bw;
      *(LAS float*)(lds + stw) = l == 1 ? cw : ss;
      *(LAS float*)(lds + xsw) = x1;
      BAR();
      if (w == 0) ST128(canon4(WSUM(r2r)), rws, gaw, 0, AUX_SC1);
      if (w == 1) { f32x4 q = *(LAS f32x4*)(lds + L_ST + (l & 3) * 16); q[3] = 0.f; ST128(canon4(q), rws, gaw, 0, AUX_SC1); }
      if (ffn) {
        u32x4 P[4];
        POLL(PA, max(max(max4(P[0]), max4(P[1])), max(max4(P[2]), max4(P[3]))) == SENT, pao, pas)
        const f32x4 pa = asf(P[0]), st = asf(P[1]);
        const float c0 = __uint_as_float(__builtin_amdgcn_readfirstlane(P[1][0]));
        const float dc = st[0] - c0;
        float t1 = fmaf(32.f, dc, st[1]), t2 = fmaf(dc, st[1] + t1, st[2]);
        const float Av = RED4(pa), Bv = RED4(asf(P[2])), Cv = RED4(asf(P[3]));
        t1 += dpp<XOR1>(t1); t2 += dpp<XOR1>(t2); t1 += dpp<XOR2>(t1); t2 += dpp<XOR2>(t2);
        t1 += dpp<ROR8>(t1); t2 += dpp<ROR8>(t2); t1 += dpp<ROR4>(t1); t2 += dpp<ROR4>(t2);
        float m, den;
        ln_fin(c0, t1, t2, m, den);
        const float gu = rbf(fmaf(fmaf(-m, Bv, Av), rcp(den), Cv));
        float og = dpp<ROR8>(gu);
        PIN(og);
        const float gg = lc & 8 ? og : gu, uu = lc & 8 ? gu : og;
        const float z = gg * fmaf(gg * gg, -2.f * L2E * 0.7978845608028654f * 0.044715f, -2.f * L2E * 0.7978845608028654f);
        const float act = rbf(gg * rcp(1.f + __builtin_amdgcn_exp2f(z))) * uu;
        const u32 av = pk2(act, act);
        f32x4 d0 = {0.f, 0.f, 0.f, 0.f}, d1 = d0;
        MV8(wdv)
        ST128(canon4(d0 + d1), rws, pdo, 0, AUX_SC1);
        if (s < 4 && w == 0) {
          const int p3o = WS_PD + (fin >> 1) * (56 * 128) + (fin & 1) * 64 + (l >> 2) * 128 + (l & 3) * 16, p3x = l < 32 ? p3o : OOB;
          int outo = lc < 4 ? oc * 2 : OOB;
          float xo = *(LAS float*)(lds + L_XS + (oc & 127) * 4);
          PIN(outo); PIN(xo);
          f32x4 v;
          __builtin_amdgcn_s_sleep(6);
          PA_KEEP();
          PF_SUMS(v, p3o, p3x)
          float yy = RED4(v);
          yy += dpp<ROR8>(yy); yy += dpp<ROR4>(yy);
          __builtin_amdgcn_raw_buffer_store_b16(__builtin_bit_cast(u16, (__bf16)(xo + rbf(gbv * rbf(yy)))), mk(gout, 1024), outo, 0, 0);
          if (b == 0) ST32(tag, rws, l == 0 ? WS_EPOCH : OOB, 0, AUX_SC1);
        } else {
          PA_KEEP();
        }
      }
    }
  }
}

extern "C" void kernel_launch(void* const* d_in, const int* in_sizes, int n_in, void* d_out, int out_size, void* d_ws, size_t ws_size, hipStream_t stream) {
  (void)hipFuncSetAttribute((const void*)fused_block, hipFuncAttributeMaxDynamicSharedMemorySize, L_END);
  fused_block<<<256, 768, L_END, stream>>>((const u16*)d_in[0], (const u16*)d_in[1], (const u16*)d_in[2], (const u16*)d_in[3], (const u16*)d_in[4],
                                           (const u16*)d_in[6], (const u16*)d_in[7], (const u16*)d_in[8], (const u16*)d_in[9], (const u16*)d_in[10],
                                           (u16*)d_out, (unsigned char*)d_ws);
}
```
